```python
import torch

def _softmax(x, axis=-1):
    return torch.softmax(x, dim=axis)
def _gelu(x):
    return torch.nn.functional.gelu(x, approximate="tanh")
def _silu(x):
    return torch.nn.functional.silu(x)
def _sigmoid(x):
    return torch.sigmoid(x)
def _layernorm(x, axis=-1, eps=1e-5):
    m = x.mean(dim=axis, keepdim=True)
    v = x.var(dim=axis, keepdim=True, unbiased=False)
    return (x - m) / torch.sqrt(v + eps)
def _scatter_add(tgt, idx, src, axis=0):
    return tgt.clone().index_add_(axis, idx, src)
def _segment_sum(src, seg, n):
    out = torch.zeros((n,) + src.shape[1:], dtype=src.dtype)
    return out.index_add_(0, seg, src)
def _pad(x, pw):
    flat = tuple(v for pair in reversed(pw) for v in pair)
    return torch.nn.functional.pad(x, flat)

kernel_name = 'standalone__linear_kernel_rfa__prompt'

def setup_inputs():

    _w = (lambda a: (lambda t: t.to(torch.bfloat16) if t.is_floating_point() else t)(torch.as_tensor(a)))
    B, H, Sq, Sk, D, M = 1, 32, 2048, 2048, 192, 16
    return {
        "q": _w((lambda *s: torch.randn(*s, dtype=torch.bfloat16) * 0.1)(B, H, Sq, D)),
        "k": _w((lambda *s: torch.randn(*s, dtype=torch.bfloat16) * 1.6)(B, H, Sk, D)),
        "v": _w((lambda *s: torch.randn(*s, dtype=torch.bfloat16) * 1.6)(B, H, Sk, D)),
        "omega": _w((lambda *s: torch.randn(*s, dtype=torch.bfloat16) * 1.6)(M, D)),
    }

def reference(q, k, v, omega):
    d = q.shape[-1]
    m = omega.shape[0]
    scale = float(d) ** -0.25
    qs = q * scale
    ks = k * scale
    q_proj = torch.einsum("bhsd,md->bhsm", qs, omega)
    k_proj = torch.einsum("bhsd,md->bhsm", ks, omega)
    q_sq = 0.5 * torch.einsum("bhsd,bhsd->bhs", qs, qs)[..., None]
    k_sq = 0.5 * torch.einsum("bhsd,bhsd->bhs", ks, ks)[..., None]
    inv_sqrt_m = float(m) ** -0.5
    phi_q = torch.exp(q_proj - q_sq) * inv_sqrt_m + 1e-6
    phi_k = torch.exp(k_proj - k_sq) * inv_sqrt_m + 1e-6
    kv = torch.einsum("bhsm,bhsd->bhmd", phi_k, v)
    z  = torch.einsum("bhsm->bhm", phi_k)
    num = torch.einsum("bhqm,bhmd->bhqd", phi_q, kv)
    den = torch.einsum("bhqm,bhm->bhq", phi_q, z)[..., None] + 1e-6
    return num / den
```

```cpp
#include <hip/hip_runtime.h>
#include <stdint.h>
#include <type_traits>

typedef __attribute__((ext_vector_type(8))) __bf16 bf16x8;
typedef __attribute__((ext_vector_type(4))) short s16x4;
typedef __attribute__((ext_vector_type(2))) __bf16 bf16x2;
typedef __attribute__((ext_vector_type(4))) float f32x4;
typedef __attribute__((ext_vector_type(2))) float f32x2;
typedef __attribute__((ext_vector_type(2))) unsigned u32x2;
typedef __attribute__((ext_vector_type(4))) unsigned u32x4;
typedef __attribute__((ext_vector_type(8))) unsigned u32x8;
typedef __attribute__((ext_vector_type(16))) unsigned u32x16;
typedef __attribute__((address_space(4))) const unsigned long long* KARG;
#define LAS __attribute__((address_space(3)))
#define SB() __builtin_amdgcn_sched_barrier(0)
#define BAR() asm volatile("s_waitcnt lgkmcnt(0)\n\ts_barrier" ::: "memory")

constexpr int H = 32, S = 2048, D = 192;
constexpr unsigned ROWB = D * 2;
constexpr unsigned NB = (unsigned)H * S * ROWB;
constexpr int NCH = 8;
constexpr int NTHR = 11 * 64;
constexpr float SCALE = (float)0.2686424829558855;
#define PV0 70
#define PVD 46
#define QD 32
constexpr unsigned TILEB = 16 * ROWB;
constexpr unsigned L_OM = 16 * TILEB;
constexpr unsigned L_PHI = L_OM + TILEB;
constexpr unsigned L_KVF = L_PHI + 8192;
constexpr unsigned L_TK = L_KVF + 6 * 1024 + 512;
constexpr unsigned L_END = L_TK + 16;
constexpr unsigned PWB = 2048;
constexpr unsigned PARTB = 3 * PWB + 128;
constexpr unsigned W_HS = H * NCH * PARTB;
constexpr unsigned W_TK = W_HS + H * 3 * 128;
constexpr unsigned W_END = W_TK + 128;
constexpr unsigned OOB = 0x40000000u;
constexpr unsigned CODE_BYTES = 11136;
constexpr int AUX_NT = 2, AUX_SC1 = 16;

__device__ __forceinline__ unsigned pk_bf16(float lo, float hi) {
    f32x2 v = {lo, hi};
    return __builtin_bit_cast(unsigned, __builtin_convertvector(v, bf16x2));
}
__device__ __forceinline__ float bf_lo(unsigned u) { return __uint_as_float(u << 16); }
__device__ __forceinline__ float bf_hi(unsigned u) { return __uint_as_float(u & 0xffff0000u); }
__device__ __forceinline__ f32x4 rbf4(f32x4 x) {
    const unsigned a = pk_bf16(x[0], x[1]), b = pk_bf16(x[2], x[3]);
    return f32x4{bf_lo(a), bf_hi(a), bf_lo(b), bf_hi(b)};
}
__device__ __forceinline__ u32x4 scale4(u32x4 u, float sc) {
    f32x2 p[4];
#pragma unroll
    for (int c = 0; c < 4; ++c) p[c] = f32x2{bf_lo(u[c]), bf_hi(u[c])};
    SB();
#pragma unroll
    for (int c = 0; c < 4; ++c) p[c] *= f32x2{sc, sc};
    SB();
    return u32x4{pk_bf16(p[0][0], p[0][1]), pk_bf16(p[1][0], p[1][1]), pk_bf16(p[2][0], p[2][1]), pk_bf16(p[3][0], p[3][1])};
}
__device__ __forceinline__ u32x4 pack2(f32x4 a, f32x4 b) {
    return u32x4{pk_bf16(a[0], a[1]), pk_bf16(a[2], a[3]), pk_bf16(b[0], b[1]), pk_bf16(b[2], b[3])};
}
__device__ __forceinline__ f32x4 mfma32(u32x4 a, u32x4 b, f32x4 c) {
    return __builtin_amdgcn_mfma_f32_16x16x32_bf16(__builtin_bit_cast(bf16x8, a), __builtin_bit_cast(bf16x8, b), c, 0, 0, 0);
}
__device__ __forceinline__ f32x4 mfma16(u32x2 a, u32x2 b, f32x4 c) {
    return __builtin_amdgcn_mfma_f32_16x16x16bf16_1k(__builtin_bit_cast(s16x4, a), __builtin_bit_cast(s16x4, b), c, 0, 0, 0);
}
template <int MASK> __device__ __forceinline__ unsigned ror8(unsigned old, unsigned src) {
    return (unsigned)__builtin_amdgcn_update_dpp((int)old, (int)src, 0x128, 0xf, MASK, false);
}
template <int CTRL> __device__ __forceinline__ float dppf(float x) {
    return __builtin_bit_cast(float, __builtin_amdgcn_mov_dpp(__builtin_bit_cast(int, x), CTRL, 0xf, 0xf, true));
}
__device__ __forceinline__ unsigned lane_id() {
    unsigned l;
    asm volatile("v_mbcnt_lo_u32_b32 %0, -1, 0\n\tv_mbcnt_hi_u32_b32 %0, -1, %0" : "=v"(l));
    return l;
}
template <int N> __device__ __forceinline__ void dma_one(__amdgpu_buffer_rsrc_t r, unsigned ldsaddr, unsigned soff, unsigned vA, unsigned vB) {
    constexpr int T = N / 6, LC = (N % 6) / 2, RG = N & 1;
    asm volatile(
        "s_mov_b32 m0, %0\n\ts_nop 0\n\t"
        "buffer_load_dwordx4 %1, %2, %3 offen offset:%4 nt lds"
        :: "s"(ldsaddr + T * TILEB + LC * 896), "v"(RG ? vB : vA), "s"(r), "s"(soff + T * TILEB), "i"(RG * 3072 + LC * 128) : "m0", "memory");
}
__device__ __forceinline__ u32x8 flags_load(__amdgpu_buffer_rsrc_t r, unsigned off) {
    u32x8 x;
    asm volatile("s_buffer_load_dwordx8 %0, %1, %2 glc\n\ts_waitcnt lgkmcnt(0)" : "=&s"(x) : "s"(r), "s"(off) : "memory");
    return x;
}
__device__ __forceinline__ void pace(unsigned t0, unsigned dt) {
    unsigned d;
    asm volatile("s_branch 2f\n1:\n\ts_sleep 1\n2:\n\ts_memrealtime vcc\n\ts_waitcnt lgkmcnt(0)\n\ts_sub_u32 %0, vcc_lo, %1\n\ts_cmp_lt_u32 %0, %2\n\ts_cbranch_scc1 1b"
                 : "=&s"(d) : "s"(t0), "i"(dt) : "vcc", "scc", "memory");
}
__device__ __forceinline__ void lds_wait_ge(unsigned addr, unsigned n) {
    unsigned c;
    asm volatile("s_branch 2f\n1:\n\ts_sleep 1\n2:\n\tds_read_b32 %0, %1\n\ts_waitcnt lgkmcnt(0)\n\tv_cmp_gt_u32 vcc, %2, %0\n\ts_cbranch_vccnz 1b"
                 : "=&v"(c) : "v"(addr), "v"(n) : "vcc", "memory");
}
template <int N> __device__ __forceinline__ void wait_vm() { SB(); asm volatile("s_waitcnt vmcnt(%0)" :: "i"(N) : "memory"); }
template <int B, int E, class F> __device__ __forceinline__ void static_for(F&& f) {
    if constexpr (B < E) { f(std::integral_constant<int, B>{}); static_for<B + 1, E>(f); }
}

__global__ void __launch_bounds__(NTHR, 3) rfa_fused(const void* Q_, const void* K_, const void* V_, const void* OM_, void* out_, void* ws_) {
    __shared__ __attribute__((aligned(16))) unsigned char lds[L_END];
    KARG ka = (KARG)__builtin_amdgcn_kernarg_segment_ptr();
    {
        unsigned vA, vB, s0_, s1_;
        asm volatile(
            "v_readfirstlane_b32 %[t0], %[tid]\n\t"
            "s_cmpk_lt_u32 %[t0], 192\n\t"
            "s_cselect_b32 %[t1], 24, 8\n\t"
            "s_load_dwordx2 s[40:41], %[ka], %[t1]\n\t"
            "v_bfe_u32 %[vA], %[tid], 3, 3\n\t"
            "v_lshlrev_b32 %[vB], 4, %[tid]\n\t"
            "v_mul_u32_u24 %[vA], 0x180, %[vA]\n\t"
            "v_bitop3_b32 %[vB], %[vB], %[tid], 48 bitop3:0x78\n\t"
            "v_and_b32 %[vB], 0x70, %[vB]\n\t"
            "v_or_b32 %[vA], %[vA], %[vB]\n\t"
            "v_xor_b32 %[vB], 64, %[vA]\n\t"
            "s_mov_b32 s42, -1\n\t"
            "s_bfm_b32 s43, 1, 17\n\t"
            "s_cbranch_scc1 2f\n\t"
            "s_lshr_b32 %[t0], %[t0], 6\n\t"
            "s_and_b32 %[t1], %[wg], 7\n\t"
            "s_addk_i32 %[t0], -3\n\t"
            "s_lshl_b32 %[t1], %[t1], 8\n\t"
            "s_add_u32 %[t1], %[t1], %[t0]\n\t"
            "s_mulk_i32 %[t0], 0x3000\n\t"
            "s_mov_b32 m0, %[t0]\n\t"
            "s_andn2_b32 %[t0], %[wg], 7\n\t"
            "s_add_u32 %[t1], %[t1], %[t0]\n\t"
            "s_mulk_i32 %[t1], 0x3000\n\t"
            "s_waitcnt lgkmcnt(0)\n\t"
            "buffer_load_dwordx4 %[vA], s[40:43], %[t1] offen nt lds\n\t"
            "buffer_load_dwordx4 %[vB], s[40:43], %[t1] offen offset:3072 nt lds\n\t"
            "s_addk_i32 m0, 896\n\ts_nop 0\n\t"
            "buffer_load_dwordx4 %[vA], s[40:43], %[t1] offen offset:128 nt lds\n\t"
            "buffer_load_dwordx4 %[vB], s[40:43], %[t1] offen offset:3200 nt lds\n\t"
            "s_addk_i32 m0, 896\n\ts_nop 0\n\t"
            "buffer_load_dwordx4 %[vA], s[40:43], %[t1] offen offset:256 nt lds\n\t"
            "buffer_load_dwordx4 %[vB], s[40:43], %[t1] offen offset:3328 nt lds\n\t"
            "s_addk_i32 m0, 4352\n\ts_addk_i32 %[t1], 6144\n\t"
            "buffer_load_dwordx4 %[vA], s[40:43], %[t1] offen nt lds\n\t"
            "buffer_load_dwordx4 %[vB], s[40:43], %[t1] offen offset:3072 nt lds\n\t"
            "s_addk_i32 m0, 896\n\ts_nop 0\n\t"
            "buffer_load_dwordx4 %[vA], s[40:43], %[t1] offen offset:128 nt lds\n\t"
            "buffer_load_dwordx4 %[vB], s[40:43], %[t1] offen offset:3200 nt lds\n\t"
            "s_addk_i32 m0, 896\n\ts_nop 0\n\t"
            "buffer_load_dwordx4 %[vA], s[40:43], %[t1] offen offset:256 nt lds\n\t"
            "buffer_load_dwordx4 %[vB], s[40:43], %[t1] offen offset:3328 nt lds\n\t"
            "s_branch 3f\n"
            "2:\n\t"
            "s_lshl_b32 %[t1], %[t0], 4\n\t"
            "s_add_u32 m0, %[t1], %[lom]\n\t"
            "s_lshl_b32 %[t1], %[t0], 1\n\t"
            "s_waitcnt lgkmcnt(0)\n\t"
            "buffer_load_dwordx4 %[vA], s[40:43], %[t1] offen lds\n\t"
            "buffer_load_dwordx4 %[vB], s[40:43], %[t1] offen offset:3072 lds\n"
            "3:"
            : [vA] "=&v"(vA), [vB] "=&v"(vB), [t0] "=&s"(s0_), [t1] "=&s"(s1_), [ka] "+s"(ka)
            : [tid] "v"(threadIdx.x), [wg] "s"(blockIdx.x), [lom] "i"(L_OM)
            : "s40", "s41", "s42", "s43", "m0", "scc", "memory");
    }
    const void* const Q = (const void*)ka[0];
    const void* const V = (const void*)ka[2];
    void* const out = (void*)ka[4];
    void* const ws = (void*)ka[5];
    unsigned long long pc0;
    asm volatile("s_getpc_b64 %0" : "=s"(pc0));
    const int b = blockIdx.x;
    const int head = (b & 7) * 4 + (b >> 6);
    const int chunk = (b >> 3) & 7;
    const int w = __builtin_amdgcn_readfirstlane(threadIdx.x >> 6);
    const unsigned ldsb = (unsigned)(size_t)(LAS unsigned char*)lds;
    const unsigned wgoff = ((unsigned)head * S + chunk * (S / NCH)) * ROWB;
    const f32x4 z4 = {0.f, 0.f, 0.f, 0.f};

    const unsigned hs = W_HS + (unsigned)(head * 3 + w) * 128;
    u32x8 f0;
    if (w >= 3) {
        asm volatile("" : "=s"(f0));
    } else {
        const __amdgpu_buffer_rsrc_t rp = __builtin_amdgcn_make_buffer_rsrc(ws, 0, W_END, 0x00020000);
        {
            const unsigned long long pc = (pc0 & ~255ull) - 256u + min((unsigned)((b >> 3) * 3 + w) * 128u, CODE_BYTES - 128u);
            unsigned c0, c1;
            asm volatile("s_load_dword %0, %2, 0x0\n\ts_load_dword %1, %2, 0x40\n\ts_waitcnt lgkmcnt(0)" : "=&s"(c0), "=&s"(c1) : "s"(pc) : "memory");
        }
        f0 = flags_load(rp, hs);
        wait_vm<0>();
        if (w == 0) *(u32x4*)(lds + L_TK) = u32x4{0u, 0u, 0u, 0u};
    }
    BAR();
    int w2 = w;
    asm volatile("" : "+s"(w2));

    if (w2 >= 3) {
        const int kw = w - 3;
        const unsigned l = lane_id();
        const unsigned i = l & 15, g = l >> 4;
        const unsigned vA = (l >> 3) * ROWB + (((l & 7) ^ (l >> 4)) << 4), vB = vA ^ 64u;
        const unsigned woff = wgoff + kw * 2 * TILEB, lws = kw * 2 * TILEB;
        const unsigned fe = (i >> 3) * 3072 + (i & 7) * 128 + ((g ^ (i >> 1)) << 4), fo = fe ^ 64u;
        const unsigned phw = L_PHI + kw * 1024 + i * 32 + g * 8;
        u32x4 wf[6];
#pragma unroll
        for (int t = 0; t < 6; ++t) wf[t] = *(const u32x4*)(lds + L_OM + ((t & 1) ? fo : fe) + (t >> 1) * 1024);
        const __amdgpu_buffer_rsrc_t rq = __builtin_amdgcn_make_buffer_rsrc((void*)Q, 0, NB, 0x00020000);
        const __amdgpu_buffer_rsrc_t ro = __builtin_amdgcn_make_buffer_rsrc(out, 0, NB, 0x00020000);
        float scale = SCALE;
        asm volatile("" : "+s"(scale));
        u32x2 zf, pq;
        u32x4 kf[6];
        auto out_tile = [&](const int T) __attribute__((always_inline)) {
            const unsigned vo1 = woff + T * TILEB + g * 4 * ROWB + i * 16;
            unsigned vo2 = woff + T * TILEB + (g * 4 + (i >> 3)) * ROWB + 256 + (i & 7) * 16;
            f32x4 dn = mfma16(pq, zf, z4);
            SB();
            f32x4 n[12];
#pragma unroll
            for (int t = 4; t < 6; ++t) {
                n[2 * t] = mfma16(pq, u32x2{kf[t][0], kf[t][1]}, z4);
                n[2 * t + 1] = mfma16(pq, u32x2{kf[t][2], kf[t][3]}, z4);
            }
            SB();
            dn = rbf4(dn);
            dn = rbf4(dn + 1e-6f);
            float rd[4];
#pragma unroll
            for (int r = 0; r < 4; ++r) rd[r] = __builtin_amdgcn_rcpf(dn[r]);
            SB();
            u32x4 xt[2];
#pragma unroll
            for (int r = 0; r < 4; r += 2) {
                unsigned o[2][2];
#pragma unroll
                for (int u = 0; u < 2; ++u)
#pragma unroll
                    for (int c = 0; c < 2; ++c) {
                        const unsigned y = pk_bf16(n[8 + 2 * c][r + u], n[9 + 2 * c][r + u]);
                        o[u][c] = pk_bf16(bf_lo(y) * rd[r + u], bf_hi(y) * rd[r + u]);
                    }
                xt[r / 2] = u32x4{ror8<0xC>(o[0][0], o[1][0]), ror8<0xC>(o[0][1], o[1][1]), ror8<0x3>(o[1][0], o[0][0]), ror8<0x3>(o[1][1], o[0][1])};
            }
            asm volatile("" : "+v"(xt[0]), "+v"(xt[1]), "+v"(vo2));
            if (T == 0) {
                wait_vm<0>();
                BAR();
#pragma unroll
                for (int t = 0; t < 4; ++t) kf[t] = *(const u32x4*)(lds + L_KVF + t * 1024 + l * 16);
            }
            SB();
            __builtin_amdgcn_raw_buffer_store_b128(xt[0], ro, vo2, 0, AUX_SC1);
            __builtin_amdgcn_raw_buffer_store_b128(xt[1], ro, vo2 + 2 * ROWB, 0, AUX_SC1);
            SB();
#pragma unroll
            for (int t = 0; t < 4; ++t) {
                n[2 * t] = mfma16(pq, u32x2{kf[t][0], kf[t][1]}, z4);
                n[2 * t + 1] = mfma16(pq, u32x2{kf[t][2], kf[t][3]}, z4);
            }
            SB();
#pragma unroll
            for (int r = 0; r < 4; ++r) {
                u32x4 o = scale4(u32x4{pk_bf16(n[0][r], n[1][r]), pk_bf16(n[2][r], n[3][r]), pk_bf16(n[4][r], n[5][r]), pk_bf16(n[6][r], n[7][r])}, rd[r]);
                asm volatile("" : "+v"(o));
                __builtin_amdgcn_raw_buffer_store_b128(o, ro, vo1 + r * ROWB, 0, AUX_SC1);
                SB();
            }
        };
        unsigned tk = 0;
        auto q_issue = [&](auto nc) __attribute__((always_inline)) {
            constexpr int N = decltype(nc)::value;
            pace(tk, PV0 + 8 * PVD + N * QD);
            dma_one<N>(rq, ldsb + lws, woff, vA, vB);
            if (N == 11) asm volatile("ds_add_u32 %0, %1" :: "v"(ldsb + L_TK + 8), "v"(1u) : "memory");
        };
#pragma unroll 1
        for (int it = 0;; ++it) {
            if (it == 2) {
                if ((kw == 0 || kw == 2) && (chunk == 0 || chunk == 7)) {
                    const char* tp = (const char*)(kw == 0 ? Q : (const void*)out) + wgoff + (chunk & 1) * (16 * TILEB - 128);
                    unsigned x;
                    asm volatile("s_load_dword %0, %1, 0x0\n\ts_waitcnt lgkmcnt(0)" : "=&s"(x) : "s"(tp) : "memory");
                }
                BAR();
                tk = __builtin_amdgcn_readfirstlane(*(const unsigned*)(lds + L_TK));
                static_for<0, 4>(q_issue);
            }
            if (it >= 3) {
                if (it == 3) {
                    static_for<10, 12>(q_issue);
                    lds_wait_ge(ldsb + L_TK + 4, 1u);
                    zf = *(const u32x2*)(lds + L_KVF + 6144 + l * 8);
                    kf[4] = *(const u32x4*)(lds + L_KVF + 4 * 1024 + l * 16);
                    kf[5] = *(const u32x4*)(lds + L_KVF + 5 * 1024 + l * 16);
                    __builtin_amdgcn_s_setprio(2);
                } else {
                    __builtin_amdgcn_s_setprio(0);
                }
                out_tile(it - 3);
                if (it == 4) break;
            }
            f32x4 pr = z4, gr = z4;
            const unsigned tb = lws + (it & 1) * TILEB;
            static_for<0, 3>([&](auto jc) __attribute__((always_inline)) {
                constexpr int J = decltype(jc)::value;
                if (it == 2) { static_for<4 + 2 * J, 6 + 2 * J>(q_issue); wait_vm<4>(); }
                wait_vm<10 - 2 * J>();
                if (it == 1) wait_vm<4 - 2 * J>();
                const u32x4 r0 = *(const u32x4*)(lds + tb + fe + J * 1024);
                const u32x4 r1 = *(const u32x4*)(lds + tb + fo + J * 1024);
                const u32x4 x0 = scale4(r0, scale);
                pr = mfma32(wf[2 * J], x0, pr);
                gr = mfma32(x0, x0, gr);
                const u32x4 x1 = scale4(r1, scale);
                gr = mfma32(x1, x1, gr);
                pr = mfma32(wf[2 * J + 1], x1, pr);
            });
            float dg = (i & 2) ? ((i & 1) ? gr[3] : gr[2]) : ((i & 1) ? gr[1] : gr[0]);
            dg = ((i >> 2) == g) ? dg : 0.f;
            const f32x4 sqb = __builtin_amdgcn_mfma_f32_16x16x4f32(1.0f, dg, z4, 0, 0, 0);
            const f32x4 pj = rbf4(pr);
            const float sq = bf_lo(pk_bf16(sqb[0], sqb[0]));
            f32x4 x;
#pragma unroll
            for (int r = 0; r < 4; ++r) x[r] = __builtin_fmaf(sq, -0.5f, pj[r]);
            x = rbf4(x);
#pragma unroll
            for (int r = 0; r < 4; ++r) x[r] = __builtin_amdgcn_exp2f(x[r] * 1.4426950408889634f);
            x = rbf4(x);
#pragma unroll
            for (int r = 0; r < 4; ++r) x[r] = __builtin_fmaf(x[r], 0.25f, 1e-6f);
            pq = u32x2{pk_bf16(x[0], x[1]), pk_bf16(x[2], x[3])};
            if (it < 2) *(u32x2*)(lds + phw + it * 512) = pq;
        }
        __builtin_amdgcn_endpgm();
    } else {
        const unsigned l = lane_id();
        const unsigned i = l & 15, g = l >> 4;
        const int lc = w;
        const __amdgpu_buffer_rsrc_t rp = __builtin_amdgcn_make_buffer_rsrc(ws, 0, W_END, 0x00020000);
        const __amdgpu_buffer_rsrc_t rv = __builtin_amdgcn_make_buffer_rsrc((void*)V, 0, NB, 0x00020000);
        const unsigned vv = (4 * g + (i >> 3)) * ROWB + (i & 7) * 16;
        const unsigned sv = wgoff + lc * 128;
        unsigned tk0 = (unsigned)__builtin_amdgcn_s_memrealtime();
        asm volatile("buffer_store_dword %0, %1, %2, 0 offen sc1"
                     :: "v"(tk0), "v"((l == 0 && lc == 0 && (b & 0x78) == 0) ? W_TK + ((b & 7) | ((b >> 7) << 3)) * 4 : OOB), "s"(rp) : "memory");
        u32x4 L[8][4];
        auto prep = [&](const int ks) __attribute__((always_inline)) {
            u32x4 bf[4];
#pragma unroll
            for (int p = 0; p < 4; ++p)
#pragma unroll
                for (int h = 0; h < 2; ++h) {
                    const unsigned X = ror8<0xC>(L[ks][p][h], L[ks][p][2 + h]);
                    const unsigned Y = ror8<0x3>(L[ks][p][2 + h], L[ks][p][h]);
                    bf[2 * h][p] = __builtin_amdgcn_perm(Y, X, 0x05040100u);
                    bf[2 * h + 1][p] = __builtin_amdgcn_perm(Y, X, 0x07060302u);
                }
#pragma unroll
            for (int e = 0; e < 4; ++e) L[ks][e] = bf[e];
            asm volatile("" : "+v"(L[ks][0]), "+v"(L[ks][1]), "+v"(L[ks][2]), "+v"(L[ks][3]));
        };
        SB();
#pragma unroll
        for (int ks = 0; ks < 8; ++ks) {
            pace(tk0, PV0 + ks * PVD);
#pragma unroll
            for (int a = 0; a < 4; ++a)
                L[ks][a] = __builtin_amdgcn_raw_buffer_load_b128(rv, vv + (a & 1) * 2 * ROWB, sv + ((ks + chunk) & 7) * 2 * TILEB + (a >> 1) * TILEB, AUX_NT);
            SB();
            if (ks == 2) {
                u32x16 ts;
                asm volatile("s_buffer_load_dwordx16 %0, %1, %2\n\ts_waitcnt lgkmcnt(0)" : "=&s"(ts) : "s"(rp), "s"(W_TK) : "memory");
                unsigned sum = 0;
#pragma unroll
                for (int p = 0; p < 16; ++p) { const unsigned d = ts[p] - tk0 + 256u; sum += d < 512u ? d : 256u; }
                tk0 += (sum >> 4) - 256u;
                if (lc == 0) *(unsigned*)(lds + L_TK) = tk0;
                SB();
            }
            if (ks >= 2 && ks < 7) { prep(ks - 2); SB(); }
        }
        BAR();
        __builtin_amdgcn_s_setprio(3);
        f32x4 acc[4] = {z4, z4, z4, z4}, accz = z4;
        const u32x4 ones = {0x3f803f80u, 0x3f803f80u, 0x3f803f80u, 0x3f803f80u};
#pragma unroll
        for (int ks = 0; ks < 8; ++ks) {
            LAS unsigned char* pp = (LAS unsigned char*)lds + L_PHI + ((ks + chunk) & 7) * 1024 + l * 8;
            const u32x2 p0 = __builtin_bit_cast(u32x2, __builtin_amdgcn_ds_read_tr16_b64_v4i16((LAS s16x4*)pp));
            const u32x2 p1 = __builtin_bit_cast(u32x2, __builtin_amdgcn_ds_read_tr16_b64_v4i16((LAS s16x4*)(pp + 512)));
            const u32x4 pa = {p0[0], p0[1], p1[0], p1[1]};
            if (ks >= 5) { SB(); prep(ks); }
            accz = mfma32(pa, ones, accz);
#pragma unroll
            for (int e = 0; e < 4; ++e) acc[e] = mfma32(pa, L[ks][e], acc[e]);
        }
        const unsigned ph = (unsigned)head * NCH * PARTB;
        const unsigned pv = ph + chunk * PARTB + lc * PWB + l * 16;
        const unsigned pz = (i <= 1 && lc == 2) ? ph + chunk * PARTB + 3 * PWB + i * 64 + g * 16 : OOB;
#pragma unroll
        for (int q = 0; q < 2; ++q) __builtin_amdgcn_raw_buffer_store_b128(pack2(acc[2 * q], acc[2 * q + 1]), rp, pv + q * 1024, 0, AUX_SC1);
        __builtin_amdgcn_raw_buffer_store_b128(__builtin_bit_cast(u32x4, accz), rp, pz, 0, AUX_SC1);
        unsigned fown = f0[0];
#pragma unroll
        for (int p = 1; p < 8; ++p) fown = (chunk == p) ? f0[p] : fown;
        const unsigned fl_wr = l == 0 ? hs + chunk * 4 : OOB;
        unsigned a0 = (lc < 2) ? (i >> 3) * 2048 + (16 * g + 8 * lc + (i & 7)) * 16 : 4 * 1024 + l * 16;
        asm volatile("" : "+v"(a0));
        u32x2 eye = {(i >> 1) == 2 * g ? ((i & 1) ? 0x3f800000u : 0x3f80u) : 0u, (i >> 1) == 2 * g + 1 ? ((i & 1) ? 0x3f800000u : 0x3f80u) : 0u};
        asm volatile("" : "+v"(eye));
        wait_vm<0>();
        __builtin_amdgcn_raw_buffer_store_b32(fown + 1u, rp, fl_wr, 0, AUX_SC1);
#pragma unroll 1
        for (unsigned spins = 0; spins < (1u << 18); ++spins) {
            const u32x8 f = flags_load(rp, hs);
            bool all = true;
#pragma unroll
            for (int p = 0; p < 8; ++p) all = all && (f[p] != f0[p]);
            if (all) break;
        }
        __builtin_amdgcn_raw_buffer_store_b32(fown + 2u, rp, fl_wr, 0, AUX_SC1);
        SB();
        u32x4 G[7][2];
        accz = __builtin_bit_cast(f32x4, __builtin_amdgcn_raw_buffer_load_b128(rp, lc == 2 ? (l & 7) * PARTB + g * 16 : OOB, ph + 3 * PWB, AUX_SC1));
        SB();
        if (lc < 2) lds_wait_ge(ldsb + L_TK + 8, 8u * 64u);
#pragma unroll
        for (int p = 0; p < 7; ++p) {
            const unsigned po = ph + ((chunk + 1 + p) & 7) * PARTB;
            SB();
#pragma unroll
            for (int e = 0; e < 2; ++e) G[p][e] = __builtin_amdgcn_raw_buffer_load_b128(rp, l * 16 + e * 1024, po + lc * PWB, AUX_SC1);
        }
        asm volatile("" ::: "memory");
        SB();
#pragma unroll
        for (int r = 0; r < 4; ++r) {
            accz[r] += dppf<0xB1>(accz[r]);
            accz[r] += dppf<0x4E>(accz[r]);
            accz[r] += dppf<0x141>(accz[r]);
        }
        if (lc == 2) *(u32x2*)(lds + L_KVF + 6144 + l * 8) = u32x2{pk_bf16(accz[0], accz[1]), pk_bf16(accz[2], accz[3])};
#pragma unroll
        for (int p = 0; p < 7; ++p)
#pragma unroll
            for (int e = 0; e < 4; ++e) {
                const unsigned u0 = G[p][e >> 1][2 * (e & 1)], u1 = G[p][e >> 1][2 * (e & 1) + 1];
                if (e < 3) acc[e] = mfma16(eye, u32x2{u0, u1}, acc[e]);
                else acc[e] += f32x4{bf_lo(u0), bf_hi(u0), bf_lo(u1), bf_hi(u1)};
            }
#pragma unroll
        for (int q = 0; q < 2; ++q) *(u32x4*)(lds + L_KVF + a0 + q * 1024) = pack2(acc[2 * q], acc[2 * q + 1]);
        if (lc == 2) {
            asm volatile("s_waitcnt lgkmcnt(0)\n\tds_write_b32 %0, %1" :: "v"(ldsb + L_TK + 4), "v"(1u) : "memory");
        } else {
            BAR();
        }
    }
}

extern "C" void kernel_launch(void* const* d_in, const int* in_sizes, int n_in,
                              void* d_out, int out_size, void* d_ws, size_t ws_size,
                              hipStream_t stream) {
    rfa_fused<<<H * NCH, NTHR, 0, stream>>>(d_in[0], d_in[1], d_in[2], d_in[3], d_out, d_ws);
}
```

```cpp
#include <hip/hip_runtime.h>
#include <stdint.h>
#include <type_traits>

typedef __attribute__((ext_vector_type(8))) __bf16 bf16x8;
typedef __attribute__((ext_vector_type(4))) short s16x4;
typedef __attribute__((ext_vector_type(2))) __bf16 bf16x2;
typedef __attribute__((ext_vector_type(4))) float f32x4;
typedef __attribute__((ext_vector_type(2))) float f32x2;
typedef __attribute__((ext_vector_type(2))) unsigned u32x2;
typedef __attribute__((ext_vector_type(4))) unsigned u32x4;
typedef __attribute__((ext_vector_type(8))) unsigned u32x8;
typedef __attribute__((ext_vector_type(16))) unsigned u32x16;
typedef __attribute__((address_space(4))) const unsigned long long* KARG;
#define LAS __attribute__((address_space(3)))
#define SB() __builtin_amdgcn_sched_barrier(0)
#define BAR() asm volatile("s_waitcnt lgkmcnt(0)\n\ts_barrier" ::: "memory")

constexpr int H = 32, S = 2048, D = 192;
constexpr unsigned ROWB = D * 2;
constexpr unsigned NB = (unsigned)H * S * ROWB;
constexpr int NCH = 8;
constexpr int NTHR = 11 * 64;
constexpr float SCALE = (float)0.2686424829558855;
#define PV0 65
#define PVD 46
#define QD 32
constexpr unsigned TILEB = 16 * ROWB;
constexpr unsigned L_OM = 16 * TILEB;
constexpr unsigned L_PHI = L_OM + TILEB;
constexpr unsigned L_KVF = L_PHI + 8192;
constexpr unsigned L_TK = L_KVF + 6 * 1024 + 512;
constexpr unsigned L_END = L_TK + 16;
constexpr unsigned PWB = 2048;
constexpr unsigned PARTB = 3 * PWB + 128;
constexpr unsigned W_HS = H * NCH * PARTB;
constexpr unsigned W_TK = W_HS + H * 3 * 128;
constexpr unsigned W_END = W_TK + 128;
constexpr unsigned OOB = 0x40000000u;
constexpr unsigned CODE_BYTES = 11136;
constexpr int AUX_NT = 2, AUX_SC1 = 16;

__device__ __forceinline__ unsigned pk_bf16(float lo, float hi) {
    f32x2 v = {lo, hi};
    return __builtin_bit_cast(unsigned, __builtin_convertvector(v, bf16x2));
}
__device__ __forceinline__ float bf_lo(unsigned u) { return __uint_as_float(u << 16); }
__device__ __forceinline__ float bf_hi(unsigned u) { return __uint_as_float(u & 0xffff0000u); }
__device__ __forceinline__ f32x4 rbf4(f32x4 x) {
    const unsigned a = pk_bf16(x[0], x[1]), b = pk_bf16(x[2], x[3]);
    return f32x4{bf_lo(a), bf_hi(a), bf_lo(b), bf_hi(b)};
}
__device__ __forceinline__ u32x4 scale4(u32x4 u, float sc) {
    f32x2 p[4];
#pragma unroll
    for (int c = 0; c < 4; ++c) p[c] = f32x2{bf_lo(u[c]), bf_hi(u[c])};
    SB();
#pragma unroll
    for (int c = 0; c < 4; ++c) p[c] *= f32x2{sc, sc};
    SB();
    return u32x4{pk_bf16(p[0][0], p[0][1]), pk_bf16(p[1][0], p[1][1]), pk_bf16(p[2][0], p[2][1]), pk_bf16(p[3][0], p[3][1])};
}
__device__ __forceinline__ u32x4 pack2(f32x4 a, f32x4 b) {
    return u32x4{pk_bf16(a[0], a[1]), pk_bf16(a[2], a[3]), pk_bf16(b[0], b[1]), pk_bf16(b[2], b[3])};
}
__device__ __forceinline__ f32x4 mfma32(u32x4 a, u32x4 b, f32x4 c) {
    return __builtin_amdgcn_mfma_f32_16x16x32_bf16(__builtin_bit_cast(bf16x8, a), __builtin_bit_cast(bf16x8, b), c, 0, 0, 0);
}
__device__ __forceinline__ f32x4 mfma16(u32x2 a, u32x2 b, f32x4 c) {
    return __builtin_amdgcn_mfma_f32_16x16x16bf16_1k(__builtin_bit_cast(s16x4, a), __builtin_bit_cast(s16x4, b), c, 0, 0, 0);
}
template <int MASK> __device__ __forceinline__ unsigned ror8(unsigned old, unsigned src) {
    return (unsigned)__builtin_amdgcn_update_dpp((int)old, (int)src, 0x128, 0xf, MASK, false);
}
template <int CTRL> __device__ __forceinline__ float dppf(float x) {
    return __builtin_bit_cast(float, __builtin_amdgcn_mov_dpp(__builtin_bit_cast(int, x), CTRL, 0xf, 0xf, true));
}
__device__ __forceinline__ unsigned lane_id() {
    unsigned l;
    asm volatile("v_mbcnt_lo_u32_b32 %0, -1, 0\n\tv_mbcnt_hi_u32_b32 %0, -1, %0" : "=v"(l));
    return l;
}
template <int N> __device__ __forceinline__ void dma_one(__amdgpu_buffer_rsrc_t r, unsigned ldsaddr, unsigned soff, unsigned vA, unsigned vB) {
    constexpr int T = N / 6, LC = (N % 6) / 2, RG = N & 1;
    asm volatile(
        "s_mov_b32 m0, %0\n\ts_nop 0\n\t"
        "buffer_load_dwordx4 %1, %2, %3 offen offset:%4 nt lds"
        :: "s"(ldsaddr + T * TILEB + LC * 896), "v"(RG ? vB : vA), "s"(r), "s"(soff + T * TILEB), "i"(RG * 3072 + LC * 128) : "m0", "memory");
}
__device__ __forceinline__ u32x8 flags_load(__amdgpu_buffer_rsrc_t r, unsigned off) {
    u32x8 x;
    asm volatile("s_buffer_load_dwordx8 %0, %1, %2 glc\n\ts_waitcnt lgkmcnt(0)" : "=&s"(x) : "s"(r), "s"(off) : "memory");
    return x;
}
__device__ __forceinline__ void pace(unsigned t0, unsigned dt) {
    unsigned d;
    asm volatile("s_branch 2f\n1:\n\ts_sleep 1\n2:\n\ts_memrealtime vcc\n\ts_waitcnt lgkmcnt(0)\n\ts_sub_u32 %0, vcc_lo, %1\n\ts_cmp_lt_u32 %0, %2\n\ts_cbranch_scc1 1b"
                 : "=&s"(d) : "s"(t0), "i"(dt) : "vcc", "scc", "memory");
}
__device__ __forceinline__ void lds_wait_ge(unsigned addr, unsigned n) {
    unsigned c;
    asm volatile("s_branch 2f\n1:\n\ts_sleep 1\n2:\n\tds_read_b32 %0, %1\n\ts_waitcnt lgkmcnt(0)\n\tv_cmp_gt_u32 vcc, %2, %0\n\ts_cbranch_vccnz 1b"
                 : "=&v"(c) : "v"(addr), "v"(n) : "vcc", "memory");
}
template <int N> __device__ __forceinline__ void wait_vm() { SB(); asm volatile("s_waitcnt vmcnt(%0)" :: "i"(N) : "memory"); }
template <int B, int E, class F> __device__ __forceinline__ void static_for(F&& f) {
    if constexpr (B < E) { f(std::integral_constant<int, B>{}); static_for<B + 1, E>(f); }
}

__global__ void __launch_bounds__(NTHR, 3) rfa_fused(const void* Q_, const void* K_, const void* V_, const void* OM_, void* out_, void* ws_) {
    __shared__ __attribute__((aligned(16))) unsigned char lds[L_END];
    KARG ka = (KARG)__builtin_amdgcn_kernarg_segment_ptr();
    {
        unsigned vA, vB, s0_, s1_;
        asm volatile(
            "v_readfirstlane_b32 %[t0], %[tid]\n\t"
            "s_cmpk_lt_u32 %[t0], 192\n\t"
            "s_cselect_b32 %[t1], 24, 8\n\t"
            "s_load_dwordx2 s[40:41], %[ka], %[t1]\n\t"
            "v_bfe_u32 %[vA], %[tid], 3, 3\n\t"
            "v_lshlrev_b32 %[vB], 4, %[tid]\n\t"
            "v_mul_u32_u24 %[vA], 0x180, %[vA]\n\t"
            "v_bitop3_b32 %[vB], %[vB], %[tid], 48 bitop3:0x78\n\t"
            "v_and_b32 %[vB], 0x70, %[vB]\n\t"
            "v_or_b32 %[vA], %[vA], %[vB]\n\t"
            "v_xor_b32 %[vB], 64, %[vA]\n\t"
            "s_mov_b32 s42, -1\n\t"
            "s_bfm_b32 s43, 1, 17\n\t"
            "s_cbranch_scc1 2f\n\t"
            "s_lshr_b32 %[t0], %[t0], 6\n\t"
            "s_and_b32 %[t1], %[wg], 7\n\t"
            "s_addk_i32 %[t0], -3\n\t"
            "s_lshl_b32 %[t1], %[t1], 8\n\t"
            "s_add_u32 %[t1], %[t1], %[t0]\n\t"
            "s_mulk_i32 %[t0], 0x3000\n\t"
            "s_mov_b32 m0, %[t0]\n\t"
            "s_andn2_b32 %[t0], %[wg], 7\n\t"
            "s_add_u32 %[t1], %[t1], %[t0]\n\t"
            "s_mulk_i32 %[t1], 0x3000\n\t"
            "s_waitcnt lgkmcnt(0)\n\t"
            "buffer_load_dwordx4 %[vA], s[40:43], %[t1] offen nt lds\n\t"
            "buffer_load_dwordx4 %[vB], s[40:43], %[t1] offen offset:3072 nt lds\n\t"
            "s_addk_i32 m0, 896\n\ts_nop 0\n\t"
            "buffer_load_dwordx4 %[vA], s[40:43], %[t1] offen offset:128 nt lds\n\t"
            "buffer_load_dwordx4 %[vB], s[40:43], %[t1] offen offset:3200 nt lds\n\t"
            "s_addk_i32 m0, 896\n\ts_nop 0\n\t"
            "buffer_load_dwordx4 %[vA], s[40:43], %[t1] offen offset:256 nt lds\n\t"
            "buffer_load_dwordx4 %[vB], s[40:43], %[t1] offen offset:3328 nt lds\n\t"
            "s_addk_i32 m0, 4352\n\ts_addk_i32 %[t1], 6144\n\t"
            "buffer_load_dwordx4 %[vA], s[40:43], %[t1] offen nt lds\n\t"
            "buffer_load_dwordx4 %[vB], s[40:43], %[t1] offen offset:3072 nt lds\n\t"
            "s_addk_i32 m0, 896\n\ts_nop 0\n\t"
            "buffer_load_dwordx4 %[vA], s[40:43], %[t1] offen offset:128 nt lds\n\t"
            "buffer_load_dwordx4 %[vB], s[40:43], %[t1] offen offset:3200 nt lds\n\t"
            "s_addk_i32 m0, 896\n\ts_nop 0\n\t"
            "buffer_load_dwordx4 %[vA], s[40:43], %[t1] offen offset:256 nt lds\n\t"
            "buffer_load_dwordx4 %[vB], s[40:43], %[t1] offen offset:3328 nt lds\n\t"
            "s_branch 3f\n"
            "2:\n\t"
            "s_lshl_b32 %[t1], %[t0], 4\n\t"
            "s_add_u32 m0, %[t1], %[lom]\n\t"
            "s_lshl_b32 %[t1], %[t0], 1\n\t"
            "s_waitcnt lgkmcnt(0)\n\t"
            "buffer_load_dwordx4 %[vA], s[40:43], %[t1] offen lds\n\t"
            "buffer_load_dwordx4 %[vB], s[40:43], %[t1] offen offset:3072 lds\n"
            "3:"
            : [vA] "=&v"(vA), [vB] "=&v"(vB), [t0] "=&s"(s0_), [t1] "=&s"(s1_), [ka] "+s"(ka)
            : [tid] "v"(threadIdx.x), [wg] "s"(blockIdx.x), [lom] "i"(L_OM)
            : "s40", "s41", "s42", "s43", "m0", "scc", "memory");
    }
    const void* const Q = (const void*)ka[0];
    const void* const V = (const void*)ka[2];
    void* const out = (void*)ka[4];
    void* const ws = (void*)ka[5];
    unsigned long long pc0;
    asm volatile("s_getpc_b64 %0" : "=s"(pc0));
    const int b = blockIdx.x;
    const int head = (b & 7) * 4 + (b >> 6);
    const int chunk = (b >> 3) & 7;
    const int w = __builtin_amdgcn_readfirstlane(threadIdx.x >> 6);
    const unsigned ldsb = (unsigned)(size_t)(LAS unsigned char*)lds;
    const unsigned wgoff = ((unsigned)head * S + chunk * (S / NCH)) * ROWB;
    const f32x4 z4 = {0.f, 0.f, 0.f, 0.f};

    const unsigned hs = W_HS + (unsigned)(head * 3 + w) * 128;
    u32x8 f0;
    if (w >= 3) {
        asm volatile("" : "=s"(f0));
    } else {
        const __amdgpu_buffer_rsrc_t rp = __builtin_amdgcn_make_buffer_rsrc(ws, 0, W_END, 0x00020000);
        {
            const unsigned long long pc = (pc0 & ~255ull) - 256u + min((unsigned)((b >> 3) * 3 + w) * 128u, CODE_BYTES - 128u);
            unsigned c0, c1;
            asm volatile("s_load_dword %0, %2, 0x0\n\ts_load_dword %1, %2, 0x40\n\ts_waitcnt lgkmcnt(0)" : "=&s"(c0), "=&s"(c1) : "s"(pc) : "memory");
        }
        f0 = flags_load(rp, hs);
        wait_vm<0>();
        if (w == 0) *(u32x4*)(lds + L_TK) = u32x4{0u, 0u, 0u, 0u};
    }
    BAR();
    int w2 = w;
    asm volatile("" : "+s"(w2));

    if (w2 >= 3) {
        const int kw = w - 3;
        const unsigned l = lane_id();
        const unsigned i = l & 15, g = l >> 4;
        const unsigned vA = (l >> 3) * ROWB + (((l & 7) ^ (l >> 4)) << 4), vB = vA ^ 64u;
        const unsigned woff = wgoff + kw * 2 * TILEB, lws = kw * 2 * TILEB;
        const unsigned fe = (i >> 3) * 3072 + (i & 7) * 128 + ((g ^ (i >> 1)) << 4), fo = fe ^ 64u;
        const unsigned phw = L_PHI + kw * 1024 + i * 32 + g * 8;
        u32x4 wf[6];
#pragma unroll
        for (int t = 0; t < 6; ++t) wf[t] = *(const u32x4*)(lds + L_OM + ((t & 1) ? fo : fe) + (t >> 1) * 1024);
        const __amdgpu_buffer_rsrc_t rq = __builtin_amdgcn_make_buffer_rsrc((void*)Q, 0, NB, 0x00020000);
        const __amdgpu_buffer_rsrc_t ro = __builtin_amdgcn_make_buffer_rsrc(out, 0, NB, 0x00020000);
        float scale = SCALE;
        asm volatile("" : "+s"(scale));
        u32x2 zf, pq;
        u32x4 kf[6];
        auto out_tile = [&](const int T) __attribute__((always_inline)) {
            const unsigned vo1 = woff + T * TILEB + g * 4 * ROWB + i * 16;
            unsigned vo2 = woff + T * TILEB + (g * 4 + (i >> 3)) * ROWB + 256 + (i & 7) * 16;
            f32x4 dn = mfma16(pq, zf, z4);
            SB();
            f32x4 n[12];
#pragma unroll
            for (int t = 4; t < 6; ++t) {
                n[2 * t] = mfma16(pq, u32x2{kf[t][0], kf[t][1]}, z4);
                n[2 * t + 1] = mfma16(pq, u32x2{kf[t][2], kf[t][3]}, z4);
            }
            SB();
            dn = rbf4(dn);
            dn = rbf4(dn + 1e-6f);
            float rd[4];
#pragma unroll
            for (int r = 0; r < 4; ++r) rd[r] = __builtin_amdgcn_rcpf(dn[r]);
            SB();
            u32x4 xt[2];
#pragma unroll
            for (int r = 0; r < 4; r += 2) {
                unsigned o[2][2];
#pragma unroll
                for (int u = 0; u < 2; ++u)
#pragma unroll
                    for (int c = 0; c < 2; ++c) {
                        const unsigned y = pk_bf16(n[8 + 2 * c][r + u], n[9 + 2 * c][r + u]);
                        o[u][c] = pk_bf16(bf_lo(y) * rd[r + u], bf_hi(y) * rd[r + u]);
                    }
                xt[r / 2] = u32x4{ror8<0xC>(o[0][0], o[1][0]), ror8<0xC>(o[0][1], o[1][1]), ror8<0x3>(o[1][0], o[0][0]), ror8<0x3>(o[1][1], o[0][1])};
            }
            asm volatile("" : "+v"(xt[0]), "+v"(xt[1]), "+v"(vo2));
            if (T == 0) {
                wait_vm<0>();
                BAR();
#pragma unroll
                for (int t = 0; t < 4; ++t) kf[t] = *(const u32x4*)(lds + L_KVF + t * 1024 + l * 16);
            }
            SB();
            __builtin_amdgcn_raw_buffer_store_b128(xt[0], ro, vo2, 0, AUX_SC1);
            __builtin_amdgcn_raw_buffer_store_b128(xt[1], ro, vo2 + 2 * ROWB, 0, AUX_SC1);
            SB();
#pragma unroll
            for (int t = 0; t < 4; ++t) {
                n[2 * t] = mfma16(pq, u32x2{kf[t][0], kf[t][1]}, z4);
                n[2 * t + 1] = mfma16(pq, u32x2{kf[t][2], kf[t][3]}, z4);
            }
            SB();
#pragma unroll
            for (int r = 0; r < 4; ++r) {
                u32x4 o = scale4(u32x4{pk_bf16(n[0][r], n[1][r]), pk_bf16(n[2][r], n[3][r]), pk_bf16(n[4][r], n[5][r]), pk_bf16(n[6][r], n[7][r])}, rd[r]);
                asm volatile("" : "+v"(o));
                __builtin_amdgcn_raw_buffer_store_b128(o, ro, vo1 + r * ROWB, 0, AUX_SC1);
                SB();
            }
        };
        unsigned tk = 0;
        auto q_issue = [&](auto nc) __attribute__((always_inline)) {
            constexpr int N = decltype(nc)::value;
            pace(tk, PV0 + 8 * PVD + N * QD);
            dma_one<N>(rq, ldsb + lws, woff, vA, vB);
            if (N == 11) asm volatile("s_mov_b64 exec, 1\n\tds_add_u32 %0, %1\n\ts_mov_b64 exec, -1" :: "v"(ldsb + L_TK + 8), "v"(1u) : "memory");
        };
#pragma unroll 1
        for (int it = 0;; ++it) {
            if (it == 2) {
                if ((kw == 0 || kw == 2) && (chunk == 0 || chunk == 7)) {
                    const char* tp = (const char*)(kw == 0 ? Q : (const void*)out) + wgoff + (chunk & 1) * (16 * TILEB - 128);
                    unsigned x;
                    asm volatile("s_load_dword %0, %1, 0x0\n\ts_waitcnt lgkmcnt(0)" : "=&s"(x) : "s"(tp) : "memory");
                }
                BAR();
                tk = __builtin_amdgcn_readfirstlane(*(const unsigned*)(lds + L_TK)) + kw * 4 - 14;
                static_for<0, 4>(q_issue);
            }
            if (it >= 3) {
                if (it == 3) {
                    static_for<10, 12>(q_issue);
                    lds_wait_ge(ldsb + L_TK + 4, 1u);
                    zf = *(const u32x2*)(lds + L_KVF + 6144 + l * 8);
                    kf[4] = *(const u32x4*)(lds + L_KVF + 4 * 1024 + l * 16);
                    kf[5] = *(const u32x4*)(lds + L_KVF + 5 * 1024 + l * 16);
                    __builtin_amdgcn_s_setprio(2);
                } else {
                    __builtin_amdgcn_s_setprio(0);
                }
                out_tile(it - 3);
                if (it == 4) break;
            }
            f32x4 pr = z4, gr = z4;
            const unsigned tb = lws + (it & 1) * TILEB;
            static_for<0, 3>([&](auto jc) __attribute__((always_inline)) {
                constexpr int J = decltype(jc)::value;
                if (it == 2) { static_for<4 + 2 * J, 6 + 2 * J>(q_issue); wait_vm<4>(); }
                wait_vm<10 - 2 * J>();
                if (it == 1) wait_vm<4 - 2 * J>();
                const u32x4 r0 = *(const u32x4*)(lds + tb + fe + J * 1024);
                const u32x4 r1 = *(const u32x4*)(lds + tb + fo + J * 1024);
                const u32x4 x0 = scale4(r0, scale);
                pr = mfma32(wf[2 * J], x0, pr);
                gr = mfma32(x0, x0, gr);
                const u32x4 x1 = scale4(r1, scale);
                gr = mfma32(x1, x1, gr);
                pr = mfma32(wf[2 * J + 1], x1, pr);
            });
            float dg = (i & 2) ? ((i & 1) ? gr[3] : gr[2]) : ((i & 1) ? gr[1] : gr[0]);
            dg = ((i >> 2) == g) ? dg : 0.f;
            const f32x4 sqb = __builtin_amdgcn_mfma_f32_16x16x4f32(1.0f, dg, z4, 0, 0, 0);
            const f32x4 pj = rbf4(pr);
            const float sq = bf_lo(pk_bf16(sqb[0], sqb[0]));
            f32x4 x;
#pragma unroll
            for (int r = 0; r < 4; ++r) x[r] = __builtin_fmaf(sq, -0.5f, pj[r]);
            x = rbf4(x);
#pragma unroll
            for (int r = 0; r < 4; ++r) x[r] = __builtin_amdgcn_exp2f(x[r] * 1.4426950408889634f);
            x = rbf4(x);
#pragma unroll
            for (int r = 0; r < 4; ++r) x[r] = __builtin_fmaf(x[r], 0.25f, 1e-6f);
            pq = u32x2{pk_bf16(x[0], x[1]), pk_bf16(x[2], x[3])};
            if (it < 2) *(u32x2*)(lds + phw + it * 512) = pq;
        }
        __builtin_amdgcn_endpgm();
    } else {
        const unsigned l = lane_id();
        const unsigned i = l & 15, g = l >> 4;
        const int lc = w;
        const __amdgpu_buffer_rsrc_t rp = __builtin_amdgcn_make_buffer_rsrc(ws, 0, W_END, 0x00020000);
        const __amdgpu_buffer_rsrc_t rv = __builtin_amdgcn_make_buffer_rsrc((void*)V, 0, NB, 0x00020000);
        const unsigned vv = (4 * g + (i >> 3)) * ROWB + (i & 7) * 16;
        const unsigned sv = wgoff + lc * 128;
        unsigned tk0 = (unsigned)__builtin_amdgcn_s_memrealtime();
        asm volatile("buffer_store_dword %0, %1, %2, 0 offen sc1"
                     :: "v"(tk0), "v"((l == 0 && lc == 0 && (b & 0x78) == 0) ? W_TK + ((b & 7) | ((b >> 7) << 3)) * 4 : OOB), "s"(rp) : "memory");
        u32x4 L[8][4];
        auto prep = [&](const int ks) __attribute__((always_inline)) {
            u32x4 bf[4];
#pragma unroll
            for (int p = 0; p < 4; ++p)
#pragma unroll
                for (int h = 0; h < 2; ++h) {
                    const unsigned X = ror8<0xC>(L[ks][p][h], L[ks][p][2 + h]);
                    const unsigned Y = ror8<0x3>(L[ks][p][2 + h], L[ks][p][h]);
                    bf[2 * h][p] = __builtin_amdgcn_perm(Y, X, 0x05040100u);
                    bf[2 * h + 1][p] = __builtin_amdgcn_perm(Y, X, 0x07060302u);
                }
#pragma unroll
            for (int e = 0; e < 4; ++e) L[ks][e] = bf[e];
            asm volatile("" : "+v"(L[ks][0]), "+v"(L[ks][1]), "+v"(L[ks][2]), "+v"(L[ks][3]));
        };
        SB();
#pragma unroll
        for (int ks = 0; ks < 8; ++ks) {
            pace(tk0, PV0 + ks * PVD);
#pragma unroll
            for (int a = 0; a < 4; ++a)
                L[ks][a] = __builtin_amdgcn_raw_buffer_load_b128(rv, vv + (a & 1) * 2 * ROWB, sv + ((ks + chunk) & 7) * 2 * TILEB + (a >> 1) * TILEB, AUX_NT);
            SB();
            if (ks == 2) {
                u32x16 ts;
                asm volatile("s_buffer_load_dwordx16 %0, %1, %2\n\ts_waitcnt lgkmcnt(0)" : "=&s"(ts) : "s"(rp), "s"(W_TK) : "memory");
                unsigned sum = 0;
#pragma unroll
                for (int p = 0; p < 16; ++p) { const unsigned d = ts[p] - tk0 + 256u; sum += d < 512u ? d : 256u; }
                tk0 += (sum >> 4) - 256u;
                if (lc == 0) *(unsigned*)(lds + L_TK) = tk0;
                SB();
            }
            if (ks >= 2 && ks < 7) { prep(ks - 2); SB(); }
        }
        BAR();
        __builtin_amdgcn_s_setprio(3);
        f32x4 acc[4] = {z4, z4, z4, z4}, accz = z4;
        const u32x4 ones = {0x3f803f80u, 0x3f803f80u, 0x3f803f80u, 0x3f803f80u};
#pragma unroll
        for (int ks = 0; ks < 8; ++ks) {
            LAS unsigned char* pp = (LAS unsigned char*)lds + L_PHI + ((ks + chunk) & 7) * 1024 + l * 8;
            const u32x2 p0 = __builtin_bit_cast(u32x2, __builtin_amdgcn_ds_read_tr16_b64_v4i16((LAS s16x4*)pp));
            const u32x2 p1 = __builtin_bit_cast(u32x2, __builtin_amdgcn_ds_read_tr16_b64_v4i16((LAS s16x4*)(pp + 512)));
            const u32x4 pa = {p0[0], p0[1], p1[0], p1[1]};
            if (ks >= 5) { SB(); prep(ks); }
            accz = mfma32(pa, ones, accz);
#pragma unroll
            for (int e = 0; e < 4; ++e) acc[e] = mfma32(pa, L[ks][e], acc[e]);
        }
        const unsigned ph = (unsigned)head * NCH * PARTB;
        const unsigned pv = ph + chunk * PARTB + lc * PWB + l * 16;
        const unsigned pz = (i <= 1 && lc == 2) ? ph + chunk * PARTB + 3 * PWB + i * 64 + g * 16 : OOB;
#pragma unroll
        for (int q = 0; q < 2; ++q) __builtin_amdgcn_raw_buffer_store_b128(pack2(acc[2 * q], acc[2 * q + 1]), rp, pv + q * 1024, 0, AUX_SC1);
        __builtin_amdgcn_raw_buffer_store_b128(__builtin_bit_cast(u32x4, accz), rp, pz, 0, AUX_SC1);
        unsigned fown = f0[0];
#pragma unroll
        for (int p = 1; p < 8; ++p) fown = (chunk == p) ? f0[p] : fown;
        const unsigned fl_wr = l == 0 ? hs + chunk * 4 : OOB;
        unsigned a0 = (lc < 2) ? (i >> 3) * 2048 + (16 * g + 8 * lc + (i & 7)) * 16 : 4 * 1024 + l * 16;
        asm volatile("" : "+v"(a0));
        u32x2 eye = {(i >> 1) == 2 * g ? ((i & 1) ? 0x3f800000u : 0x3f80u) : 0u, (i >> 1) == 2 * g + 1 ? ((i & 1) ? 0x3f800000u : 0x3f80u) : 0u};
        asm volatile("" : "+v"(eye));
        wait_vm<0>();
        __builtin_amdgcn_raw_buffer_store_b32(fown + 1u, rp, fl_wr, 0, AUX_SC1);
#pragma unroll 1
        for (unsigned spins = 0; spins < (1u << 18); ++spins) {
            const u32x8 f = flags_load(rp, hs);
            bool all = true;
#pragma unroll
            for (int p = 0; p < 8; ++p) all = all && (f[p] != f0[p]);
            if (all) break;
        }
        __builtin_amdgcn_raw_buffer_store_b32(fown + 2u, rp, fl_wr, 0, AUX_SC1);
        SB();
        u32x4 G[7][2];
        accz = __builtin_bit_cast(f32x4, __builtin_amdgcn_raw_buffer_load_b128(rp, lc == 2 ? (l & 7) * PARTB + g * 16 : OOB, ph + 3 * PWB, AUX_SC1));
        SB();
        if (lc == 2) pace(tk0, PV0 + 8 * PVD + 11 * QD - 40);
        if (lc < 2) lds_wait_ge(ldsb + L_TK + 8, 8u);
#pragma unroll
        for (int p = 0; p < 7; ++p) {
            const unsigned po = ph + ((chunk + 1 + p) & 7) * PARTB;
            SB();
#pragma unroll
            for (int e = 0; e < 2; ++e) G[p][e] = __builtin_amdgcn_raw_buffer_load_b128(rp, l * 16 + e * 1024, po + lc * PWB, AUX_SC1);
        }
        asm volatile("" ::: "memory");
        SB();
#pragma unroll
        for (int r = 0; r < 4; ++r) {
            accz[r] += dppf<0xB1>(accz[r]);
            accz[r] += dppf<0x4E>(accz[r]);
            accz[r] += dppf<0x141>(accz[r]);
        }
        if (lc == 2) *(u32x2*)(lds + L_KVF + 6144 + l * 8) = u32x2{pk_bf16(accz[0], accz[1]), pk_bf16(accz[2], accz[3])};
#pragma unroll
        for (int p = 0; p < 7; ++p)
#pragma unroll
            for (int e = 0; e < 4; ++e) {
                const unsigned u0 = G[p][e >> 1][2 * (e & 1)], u1 = G[p][e >> 1][2 * (e & 1) + 1];
                if (e < 3) acc[e] = mfma16(eye, u32x2{u0, u1}, acc[e]);
                else acc[e] += f32x4{bf_lo(u0), bf_hi(u0), bf_lo(u1), bf_hi(u1)};
            }
#pragma unroll
        for (int q = 0; q < 2; ++q) *(u32x4*)(lds + L_KVF + a0 + q * 1024) = pack2(acc[2 * q], acc[2 * q + 1]);
        if (lc == 2) {
            asm volatile("s_waitcnt lgkmcnt(0)\n\tds_write_b32 %0, %1" :: "v"(ldsb + L_TK + 4), "v"(1u) : "memory");
        } else {
            BAR();
        }
    }
}

extern "C" void kernel_launch(void* const* d_in, const int* in_sizes, int n_in,
                              void* d_out, int out_size, void* d_ws, size_t ws_size,
                              hipStream_t stream) {
    rfa_fused<<<H * NCH, NTHR, 0, stream>>>(d_in[0], d_in[1], d_in[2], d_in[3], d_out, d_ws);
}
```

```cpp
#include <hip/hip_runtime.h>
#include <stdint.h>
#include <type_traits>

typedef __attribute__((ext_vector_type(8))) __bf16 bf16x8;
typedef __attribute__((ext_vector_type(4))) short s16x4;
typedef __attribute__((ext_vector_type(2))) __bf16 bf16x2;
typedef __attribute__((ext_vector_type(4))) float f32x4;
typedef __attribute__((ext_vector_type(2))) float f32x2;
typedef __attribute__((ext_vector_type(2))) unsigned u32x2;
typedef __attribute__((ext_vector_type(4))) unsigned u32x4;
typedef __attribute__((ext_vector_type(8))) unsigned u32x8;
typedef __attribute__((ext_vector_type(16))) unsigned u32x16;
typedef __attribute__((address_space(4))) const unsigned long long* KARG;
#define LAS __attribute__((address_space(3)))
#define SB() __builtin_amdgcn_sched_barrier(0)
#define BAR() asm volatile("s_waitcnt lgkmcnt(0)\n\ts_barrier" ::: "memory")

constexpr int H = 32, S = 2048, D = 192;
constexpr unsigned ROWB = D * 2;
constexpr unsigned NB = (unsigned)H * S * ROWB;
constexpr int NCH = 8;
constexpr int NTHR = 11 * 64;
constexpr float SCALE = (float)0.2686424829558855;
constexpr unsigned PV0 = 56;
constexpr unsigned PVD = 46;
constexpr unsigned QD = 32;
constexpr unsigned LIM = 40;
constexpr unsigned TILEB = 16 * ROWB;
constexpr unsigned L_OM = 16 * TILEB;
constexpr unsigned L_PHI = L_OM + TILEB;
constexpr unsigned L_KVF = L_PHI + 8192;
constexpr unsigned L_TK = L_KVF + 6 * 1024 + 512;
constexpr unsigned L_END = L_TK + 16;
constexpr unsigned PWB = 2048;
constexpr unsigned PARTB = 3 * PWB + 128;
constexpr unsigned W_HS = H * NCH * PARTB;
constexpr unsigned W_TK = W_HS + H * 3 * 128;
constexpr unsigned W_END = W_TK + 128;
constexpr unsigned OOB = 0x40000000u;
constexpr unsigned CODE_BYTES = 11392;
constexpr int AUX_NT = 2, AUX_SC1 = 16;

__device__ __forceinline__ unsigned pk_bf16(float lo, float hi) {
    f32x2 v = {lo, hi};
    return __builtin_bit_cast(unsigned, __builtin_convertvector(v, bf16x2));
}
__device__ __forceinline__ float bf_lo(unsigned u) { return __uint_as_float(u << 16); }
__device__ __forceinline__ float bf_hi(unsigned u) { return __uint_as_float(u & 0xffff0000u); }
__device__ __forceinline__ f32x4 rbf4(f32x4 x) {
    const unsigned a = pk_bf16(x[0], x[1]), b = pk_bf16(x[2], x[3]);
    return f32x4{bf_lo(a), bf_hi(a), bf_lo(b), bf_hi(b)};
}
__device__ __forceinline__ u32x4 scale4(u32x4 u, float sc) {
    f32x2 p[4];
#pragma unroll
    for (int c = 0; c < 4; ++c) p[c] = f32x2{bf_lo(u[c]), bf_hi(u[c])};
    SB();
#pragma unroll
    for (int c = 0; c < 4; ++c) p[c] *= f32x2{sc, sc};
    SB();
    return u32x4{pk_bf16(p[0][0], p[0][1]), pk_bf16(p[1][0], p[1][1]), pk_bf16(p[2][0], p[2][1]), pk_bf16(p[3][0], p[3][1])};
}
__device__ __forceinline__ u32x4 pack2(f32x4 a, f32x4 b) {
    return u32x4{pk_bf16(a[0], a[1]), pk_bf16(a[2], a[3]), pk_bf16(b[0], b[1]), pk_bf16(b[2], b[3])};
}
__device__ __forceinline__ f32x4 mfma32(u32x4 a, u32x4 b, f32x4 c) {
    return __builtin_amdgcn_mfma_f32_16x16x32_bf16(__builtin_bit_cast(bf16x8, a), __builtin_bit_cast(bf16x8, b), c, 0, 0, 0);
}
__device__ __forceinline__ f32x4 mfma16(u32x2 a, u32x2 b, f32x4 c) {
    return __builtin_amdgcn_mfma_f32_16x16x16bf16_1k(__builtin_bit_cast(s16x4, a), __builtin_bit_cast(s16x4, b), c, 0, 0, 0);
}
template <int MASK> __device__ __forceinline__ unsigned ror8(unsigned old, unsigned src) {
    return (unsigned)__builtin_amdgcn_update_dpp((int)old, (int)src, 0x128, 0xf, MASK, false);
}
template <int CTRL> __device__ __forceinline__ float dppf(float x) {
    return __builtin_bit_cast(float, __builtin_amdgcn_mov_dpp(__builtin_bit_cast(int, x), CTRL, 0xf, 0xf, true));
}
__device__ __forceinline__ unsigned lane_id() {
    unsigned l;
    asm volatile("v_mbcnt_lo_u32_b32 %0, -1, 0\n\tv_mbcnt_hi_u32_b32 %0, -1, %0" : "=v"(l));
    return l;
}
template <int N> __device__ __forceinline__ void dma_one(__amdgpu_buffer_rsrc_t r, unsigned ldsaddr, unsigned soff, unsigned vA, unsigned vB) {
    constexpr int T = N / 6, LC = (N % 6) / 2, RG = N & 1;
    asm volatile(
        "s_mov_b32 m0, %0\n\ts_nop 0\n\t"
        "buffer_load_dwordx4 %1, %2, %3 offen offset:%4 nt lds"
        :: "s"(ldsaddr + T * TILEB + LC * 896), "v"(RG ? vB : vA), "s"(r), "s"(soff + T * TILEB), "i"(RG * 3072 + LC * 128) : "m0", "memory");
}
__device__ __forceinline__ u32x8 flags_load(__amdgpu_buffer_rsrc_t r, unsigned off) {
    u32x8 x;
    asm volatile("s_buffer_load_dwordx8 %0, %1, %2 glc\n\ts_waitcnt lgkmcnt(0)" : "=&s"(x) : "s"(r), "s"(off) : "memory");
    return x;
}
__device__ __forceinline__ void pace(unsigned t0, unsigned dt) {
    unsigned d;
    asm volatile("s_branch 2f\n1:\n\ts_sleep 1\n2:\n\ts_memrealtime vcc\n\ts_waitcnt lgkmcnt(0)\n\ts_sub_u32 %0, vcc_lo, %1\n\ts_cmp_lt_u32 %0, %2\n\ts_cbranch_scc1 1b"
                 : "=&s"(d) : "s"(t0), "i"(dt) : "vcc", "scc", "memory");
}
__device__ __forceinline__ void pace_e(unsigned& t0, unsigned dt) {
    unsigned d;
    asm volatile("s_branch 2f\n1:\n\ts_sleep 1\n2:\n\ts_memrealtime vcc\n\ts_waitcnt lgkmcnt(0)\n\ts_sub_u32 %0, vcc_lo, %1\n\ts_cmp_lt_u32 %0, %2\n\ts_cbranch_scc1 1b\n\t"
                 "s_min_u32 %0, %0, %3\n\ts_sub_u32 %1, vcc_lo, %0"
                 : "=&s"(d), "+s"(t0) : "i"(dt), "i"(dt + LIM) : "vcc", "scc", "memory");
}
__device__ __forceinline__ void lds_wait_ge(unsigned addr, unsigned n) {
    unsigned c;
    asm volatile("s_branch 2f\n1:\n\ts_sleep 1\n2:\n\tds_read_b32 %0, %1\n\ts_waitcnt lgkmcnt(0)\n\tv_cmp_gt_u32 vcc, %2, %0\n\ts_cbranch_vccnz 1b"
                 : "=&v"(c) : "v"(addr), "v"(n) : "vcc", "memory");
}
template <int N> __device__ __forceinline__ void wait_vm() { SB(); asm volatile("s_waitcnt vmcnt(%0)" :: "i"(N) : "memory"); }
template <int B, int E, class F> __device__ __forceinline__ void static_for(F&& f) {
    if constexpr (B < E) { f(std::integral_constant<int, B>{}); static_for<B + 1, E>(f); }
}

__global__ void __launch_bounds__(NTHR, 3) rfa_fused(const void* Q_, const void* K_, const void* V_, const void* OM_, void* out_, void* ws_) {
    __shared__ __attribute__((aligned(16))) unsigned char lds[L_END];
    KARG ka = (KARG)__builtin_amdgcn_kernarg_segment_ptr();
    {
        unsigned vA, vB, s0_, s1_;
        asm volatile(
            "v_readfirstlane_b32 %[t0], %[tid]\n\t"
            "s_cmpk_lt_u32 %[t0], 192\n\t"
            "s_cselect_b32 %[t1], 24, 8\n\t"
            "s_load_dwordx2 s[40:41], %[ka], %[t1]\n\t"
            "v_bfe_u32 %[vA], %[tid], 3, 3\n\t"
            "v_lshlrev_b32 %[vB], 4, %[tid]\n\t"
            "v_mul_u32_u24 %[vA], 0x180, %[vA]\n\t"
            "v_bitop3_b32 %[vB], %[vB], %[tid], 48 bitop3:0x78\n\t"
            "v_and_b32 %[vB], 0x70, %[vB]\n\t"
            "v_or_b32 %[vA], %[vA], %[vB]\n\t"
            "v_xor_b32 %[vB], 64, %[vA]\n\t"
            "s_mov_b32 s42, -1\n\t"
            "s_bfm_b32 s43, 1, 17\n\t"
            "s_cbranch_scc1 2f\n\t"
            "s_lshr_b32 %[t0], %[t0], 6\n\t"
            "s_and_b32 %[t1], %[wg], 7\n\t"
            "s_addk_i32 %[t0], -3\n\t"
            "s_lshl_b32 %[t1], %[t1], 8\n\t"
            "s_add_u32 %[t1], %[t1], %[t0]\n\t"
            "s_mulk_i32 %[t0], 0x3000\n\t"
            "s_mov_b32 m0, %[t0]\n\t"
            "s_andn2_b32 %[t0], %[wg], 7\n\t"
            "s_add_u32 %[t1], %[t1], %[t0]\n\t"
            "s_mulk_i32 %[t1], 0x3000\n\t"
            "s_waitcnt lgkmcnt(0)\n\t"
            "buffer_load_dwordx4 %[vA], s[40:43], %[t1] offen nt lds\n\t"
            "buffer_load_dwordx4 %[vB], s[40:43], %[t1] offen offset:3072 nt lds\n\t"
            "s_addk_i32 m0, 896\n\ts_nop 0\n\t"
            "buffer_load_dwordx4 %[vA], s[40:43], %[t1] offen offset:128 nt lds\n\t"
            "buffer_load_dwordx4 %[vB], s[40:43], %[t1] offen offset:3200 nt lds\n\t"
            "s_addk_i32 m0, 896\n\ts_nop 0\n\t"
            "buffer_load_dwordx4 %[vA], s[40:43], %[t1] offen offset:256 nt lds\n\t"
            "buffer_load_dwordx4 %[vB], s[40:43], %[t1] offen offset:3328 nt lds\n\t"
            "s_addk_i32 m0, 4352\n\ts_addk_i32 %[t1], 6144\n\t"
            "buffer_load_dwordx4 %[vA], s[40:43], %[t1] offen nt lds\n\t"
            "buffer_load_dwordx4 %[vB], s[40:43], %[t1] offen offset:3072 nt lds\n\t"
            "s_addk_i32 m0, 896\n\ts_nop 0\n\t"
            "buffer_load_dwordx4 %[vA], s[40:43], %[t1] offen offset:128 nt lds\n\t"
            "buffer_load_dwordx4 %[vB], s[40:43], %[t1] offen offset:3200 nt lds\n\t"
            "s_addk_i32 m0, 896\n\ts_nop 0\n\t"
            "buffer_load_dwordx4 %[vA], s[40:43], %[t1] offen offset:256 nt lds\n\t"
            "buffer_load_dwordx4 %[vB], s[40:43], %[t1] offen offset:3328 nt lds\n\t"
            "s_branch 3f\n"
            "2:\n\t"
            "s_lshl_b32 %[t1], %[t0], 4\n\t"
            "s_add_u32 m0, %[t1], %[lom]\n\t"
            "s_lshl_b32 %[t1], %[t0], 1\n\t"
            "s_waitcnt lgkmcnt(0)\n\t"
            "buffer_load_dwordx4 %[vA], s[40:43], %[t1] offen lds\n\t"
            "buffer_load_dwordx4 %[vB], s[40:43], %[t1] offen offset:3072 lds\n"
            "3:"
            : [vA] "=&v"(vA), [vB] "=&v"(vB), [t0] "=&s"(s0_), [t1] "=&s"(s1_), [ka] "+s"(ka)
            : [tid] "v"(threadIdx.x), [wg] "s"(blockIdx.x), [lom] "i"(L_OM)
            : "s40", "s41", "s42", "s43", "m0", "scc", "memory");
    }
    const void* const Q = (const void*)ka[0];
    const void* const V = (const void*)ka[2];
    void* const out = (void*)ka[4];
    void* const ws = (void*)ka[5];
    unsigned long long pc0;
    asm volatile("s_getpc_b64 %0" : "=s"(pc0));
    const int b = blockIdx.x;
    const int head = (b & 7) * 4 + (b >> 6);
    const int chunk = (b >> 3) & 7;
    const int w = __builtin_amdgcn_readfirstlane(threadIdx.x >> 6);
    const unsigned ldsb = (unsigned)(size_t)(LAS unsigned char*)lds;
    const unsigned wgoff = ((unsigned)head * S + chunk * (S / NCH)) * ROWB;
    const f32x4 z4 = {0.f, 0.f, 0.f, 0.f};

    const unsigned hs = W_HS + (unsigned)(head * 3 + w) * 128;
    u32x8 f0;
    if (w >= 3) {
        asm volatile("" : "=s"(f0));
    } else {
        const __amdgpu_buffer_rsrc_t rp = __builtin_amdgcn_make_buffer_rsrc(ws, 0, W_END, 0x00020000);
        {
            const unsigned long long pc = (pc0 & ~255ull) - 256u + min((unsigned)((b >> 3) * 3 + w) * 128u, CODE_BYTES - 128u);
            unsigned c0, c1;
            asm volatile("s_load_dword %0, %2, 0x0\n\ts_load_dword %1, %2, 0x40\n\ts_waitcnt lgkmcnt(0)" : "=&s"(c0), "=&s"(c1) : "s"(pc) : "memory");
        }
        f0 = flags_load(rp, hs);
        wait_vm<0>();
        if (w == 0) *(u32x4*)(lds + L_TK) = u32x4{0u, 0u, 0u, 0u};
    }
    BAR();
    int w2 = w;
    asm volatile("" : "+s"(w2));

    if (w2 >= 3) {
        const int kw = w - 3;
        const unsigned l = lane_id();
        const unsigned i = l & 15, g = l >> 4;
        const unsigned vA = (l >> 3) * ROWB + (((l & 7) ^ (l >> 4)) << 4), vB = vA ^ 64u;
        const unsigned woff = wgoff + kw * 2 * TILEB, lws = kw * 2 * TILEB;
        const unsigned fe = (i >> 3) * 3072 + (i & 7) * 128 + ((g ^ (i >> 1)) << 4), fo = fe ^ 64u;
        const unsigned phw = L_PHI + kw * 1024 + i * 32 + g * 8;
        if (kw & 1) __builtin_amdgcn_s_sleep(6);
        if (kw & 2) __builtin_amdgcn_s_sleep(12);
        if (kw & 4) __builtin_amdgcn_s_sleep(24);
        u32x4 wf[6];
#pragma unroll
        for (int t = 0; t < 6; ++t) wf[t] = *(const u32x4*)(lds + L_OM + ((t & 1) ? fo : fe) + (t >> 1) * 1024);
        const __amdgpu_buffer_rsrc_t rq = __builtin_amdgcn_make_buffer_rsrc((void*)Q, 0, NB, 0x00020000);
        const __amdgpu_buffer_rsrc_t ro = __builtin_amdgcn_make_buffer_rsrc(out, 0, NB, 0x00020000);
        float scale = SCALE;
        asm volatile("" : "+s"(scale));
        u32x2 zf, pq;
        u32x4 kf[6];
        auto out_tile = [&](const int T) __attribute__((always_inline)) {
            const unsigned vo1 = woff + T * TILEB + g * 4 * ROWB + i * 16;
            unsigned vo2 = woff + T * TILEB + (g * 4 + (i >> 3)) * ROWB + 256 + (i & 7) * 16;
            f32x4 dn = mfma16(pq, zf, z4);
            SB();
            f32x4 n[12];
#pragma unroll
            for (int t = 4; t < 6; ++t) {
                n[2 * t] = mfma16(pq, u32x2{kf[t][0], kf[t][1]}, z4);
                n[2 * t + 1] = mfma16(pq, u32x2{kf[t][2], kf[t][3]}, z4);
            }
            SB();
            dn = rbf4(dn);
            dn = rbf4(dn + 1e-6f);
            float rd[4];
#pragma unroll
            for (int r = 0; r < 4; ++r) rd[r] = __builtin_amdgcn_rcpf(dn[r]);
            SB();
            u32x4 xt[2];
#pragma unroll
            for (int r = 0; r < 4; r += 2) {
                unsigned o[2][2];
#pragma unroll
                for (int u = 0; u < 2; ++u)
#pragma unroll
                    for (int c = 0; c < 2; ++c) {
                        const unsigned y = pk_bf16(n[8 + 2 * c][r + u], n[9 + 2 * c][r + u]);
                        o[u][c] = pk_bf16(bf_lo(y) * rd[r + u], bf_hi(y) * rd[r + u]);
                    }
                xt[r / 2] = u32x4{ror8<0xC>(o[0][0], o[1][0]), ror8<0xC>(o[0][1], o[1][1]), ror8<0x3>(o[1][0], o[0][0]), ror8<0x3>(o[1][1], o[0][1])};
            }
            asm volatile("" : "+v"(xt[0]), "+v"(xt[1]), "+v"(vo2));
            if (T == 0) {
                wait_vm<0>();
                BAR();
#pragma unroll
                for (int t = 0; t < 4; ++t) kf[t] = *(const u32x4*)(lds + L_KVF + t * 1024 + l * 16);
            }
            SB();
            __builtin_amdgcn_raw_buffer_store_b128(xt[0], ro, vo2, 0, AUX_SC1);
            __builtin_amdgcn_raw_buffer_store_b128(xt[1], ro, vo2 + 2 * ROWB, 0, AUX_SC1);
            SB();
#pragma unroll
            for (int t = 0; t < 4; ++t) {
                n[2 * t] = mfma16(pq, u32x2{kf[t][0], kf[t][1]}, z4);
                n[2 * t + 1] = mfma16(pq, u32x2{kf[t][2], kf[t][3]}, z4);
            }
            SB();
#pragma unroll
            for (int r = 0; r < 4; ++r) {
                u32x4 o = scale4(u32x4{pk_bf16(n[0][r], n[1][r]), pk_bf16(n[2][r], n[3][r]), pk_bf16(n[4][r], n[5][r]), pk_bf16(n[6][r], n[7][r])}, rd[r]);
                asm volatile("" : "+v"(o));
                __builtin_amdgcn_raw_buffer_store_b128(o, ro, vo1 + r * ROWB, 0, AUX_SC1);
                SB();
            }
        };
        unsigned tk = 0;
        auto q_issue = [&](auto nc) __attribute__((always_inline)) {
            constexpr int N = decltype(nc)::value;
            pace_e(tk, PV0 + 8 * PVD + N * QD);
            dma_one<N>(rq, ldsb + lws, woff, vA, vB);
            if (N == 11) asm volatile("s_mov_b64 exec, 1\n\tds_add_u32 %0, %1\n\ts_mov_b64 exec, -1" :: "v"(ldsb + L_TK + 8), "v"(1u) : "memory");
        };
#pragma unroll 1
        for (int it = 0;; ++it) {
            if (it == 2) {
                if ((kw == 0 || kw == 2) && (chunk == 0 || chunk == 7)) {
                    const char* tp = (const char*)(kw == 0 ? Q : (const void*)out) + wgoff + (chunk & 1) * (16 * TILEB - 128);
                    unsigned x;
                    asm volatile("s_load_dword %0, %1, 0x0\n\ts_waitcnt lgkmcnt(0)" : "=&s"(x) : "s"(tp) : "memory");
                }
                BAR();
                tk = __builtin_amdgcn_readfirstlane(*(const unsigned*)(lds + L_TK)) + kw * 4 - 14;
                static_for<0, 4>(q_issue);
            }
            if (it >= 3) {
                if (it == 3) {
                    static_for<10, 12>(q_issue);
                    lds_wait_ge(ldsb + L_TK + 4, 1u);
                    zf = *(const u32x2*)(lds + L_KVF + 6144 + l * 8);
                    kf[4] = *(const u32x4*)(lds + L_KVF + 4 * 1024 + l * 16);
                    kf[5] = *(const u32x4*)(lds + L_KVF + 5 * 1024 + l * 16);
                    __builtin_amdgcn_s_setprio(2);
                } else {
                    __builtin_amdgcn_s_setprio(0);
                }
                out_tile(it - 3);
                if (it == 4) break;
            }
            f32x4 pr = z4, gr = z4;
            const unsigned tb = lws + (it & 1) * TILEB;
            static_for<0, 3>([&](auto jc) __attribute__((always_inline)) {
                constexpr int J = decltype(jc)::value;
                if (it == 2) { static_for<4 + 2 * J, 6 + 2 * J>(q_issue); wait_vm<4>(); }
                wait_vm<10 - 2 * J>();
                if (it == 1) wait_vm<4 - 2 * J>();
                const u32x4 r0 = *(const u32x4*)(lds + tb + fe + J * 1024);
                const u32x4 r1 = *(const u32x4*)(lds + tb + fo + J * 1024);
                const u32x4 x0 = scale4(r0, scale);
                pr = mfma32(wf[2 * J], x0, pr);
                gr = mfma32(x0, x0, gr);
                const u32x4 x1 = scale4(r1, scale);
                gr = mfma32(x1, x1, gr);
                pr = mfma32(wf[2 * J + 1], x1, pr);
            });
            float dg = (i & 2) ? ((i & 1) ? gr[3] : gr[2]) : ((i & 1) ? gr[1] : gr[0]);
            dg = ((i >> 2) == g) ? dg : 0.f;
            const f32x4 sqb = __builtin_amdgcn_mfma_f32_16x16x4f32(1.0f, dg, z4, 0, 0, 0);
            const f32x4 pj = rbf4(pr);
            const float sq = bf_lo(pk_bf16(sqb[0], sqb[0]));
            f32x4 x;
#pragma unroll
            for (int r = 0; r < 4; ++r) x[r] = __builtin_fmaf(sq, -0.5f, pj[r]);
            x = rbf4(x);
#pragma unroll
            for (int r = 0; r < 4; ++r) x[r] = __builtin_amdgcn_exp2f(x[r] * 1.4426950408889634f);
            x = rbf4(x);
#pragma unroll
            for (int r = 0; r < 4; ++r) x[r] = __builtin_fmaf(x[r], 0.25f, 1e-6f);
            pq = u32x2{pk_bf16(x[0], x[1]), pk_bf16(x[2], x[3])};
            if (it < 2) *(u32x2*)(lds + phw + it * 512) = pq;
        }
        __builtin_amdgcn_endpgm();
    } else {
        const unsigned l = lane_id();
        const unsigned i = l & 15, g = l >> 4;
        const int lc = w;
        const __amdgpu_buffer_rsrc_t rp = __builtin_amdgcn_make_buffer_rsrc(ws, 0, W_END, 0x00020000);
        const __amdgpu_buffer_rsrc_t rv = __builtin_amdgcn_make_buffer_rsrc((void*)V, 0, NB, 0x00020000);
        const unsigned vv = (4 * g + (i >> 3)) * ROWB + (i & 7) * 16;
        const unsigned sv = wgoff + lc * 128;
        unsigned tk0 = (unsigned)__builtin_amdgcn_s_memrealtime();
        asm volatile("buffer_store_dword %0, %1, %2, 0 offen sc1"
                     :: "v"(tk0), "v"((l == 0 && lc == 0 && (b & 0x78) == 0) ? W_TK + ((b & 7) | ((b >> 7) << 3)) * 4 : OOB), "s"(rp) : "memory");
        u32x4 L[8][4];
        auto prep = [&](const int ks) __attribute__((always_inline)) {
            u32x4 bf[4];
#pragma unroll
            for (int p = 0; p < 4; ++p)
#pragma unroll
                for (int h = 0; h < 2; ++h) {
                    const unsigned X = ror8<0xC>(L[ks][p][h], L[ks][p][2 + h]);
                    const unsigned Y = ror8<0x3>(L[ks][p][2 + h], L[ks][p][h]);
                    bf[2 * h][p] = __builtin_amdgcn_perm(Y, X, 0x05040100u);
                    bf[2 * h + 1][p] = __builtin_amdgcn_perm(Y, X, 0x07060302u);
                }
#pragma unroll
            for (int e = 0; e < 4; ++e) L[ks][e] = bf[e];
            asm volatile("" : "+v"(L[ks][0]), "+v"(L[ks][1]), "+v"(L[ks][2]), "+v"(L[ks][3]));
        };
        SB();
#pragma unroll
        for (int ks = 0; ks < 8; ++ks) {
            if (ks > 2) pace_e(tk0, PV0 + ks * PVD); else pace(tk0, PV0 + ks * PVD);
#pragma unroll
            for (int a = 0; a < 4; ++a)
                L[ks][a] = __builtin_amdgcn_raw_buffer_load_b128(rv, vv + (a & 1) * 2 * ROWB, sv + ((ks + chunk) & 7) * 2 * TILEB + (a >> 1) * TILEB, AUX_NT);
            SB();
            if (ks == 2) {
                u32x16 ts;
                asm volatile("s_buffer_load_dwordx16 %0, %1, %2\n\ts_waitcnt lgkmcnt(0)" : "=&s"(ts) : "s"(rp), "s"(W_TK) : "memory");
                unsigned sum = 0;
#pragma unroll
                for (int p = 0; p < 16; ++p) { const unsigned d = ts[p] - tk0 + 256u; sum += d < 512u ? d : 256u; }
                tk0 += (sum >> 4) - 256u;
                if (lc == 0) *(unsigned*)(lds + L_TK) = tk0;
                SB();
            }
            if (ks >= 2 && ks < 7) { prep(ks - 2); SB(); }
        }
        BAR();
        __builtin_amdgcn_s_setprio(3);
        f32x4 acc[4] = {z4, z4, z4, z4}, accz = z4;
        const u32x4 ones = {0x3f803f80u, 0x3f803f80u, 0x3f803f80u, 0x3f803f80u};
#pragma unroll
        for (int ks = 0; ks < 8; ++ks) {
            LAS unsigned char* pp = (LAS unsigned char*)lds + L_PHI + ((ks + chunk) & 7) * 1024 + l * 8;
            const u32x2 p0 = __builtin_bit_cast(u32x2, __builtin_amdgcn_ds_read_tr16_b64_v4i16((LAS s16x4*)pp));
            const u32x2 p1 = __builtin_bit_cast(u32x2, __builtin_amdgcn_ds_read_tr16_b64_v4i16((LAS s16x4*)(pp + 512)));
            const u32x4 pa = {p0[0], p0[1], p1[0], p1[1]};
            if (ks >= 5) { SB(); prep(ks); }
            accz = mfma32(pa, ones, accz);
#pragma unroll
            for (int e = 0; e < 4; ++e) acc[e] = mfma32(pa, L[ks][e], acc[e]);
        }
        const unsigned ph = (unsigned)head * NCH * PARTB;
        const unsigned pv = ph + chunk * PARTB + lc * PWB + l * 16;
        const unsigned pz = (i <= 1 && lc == 2) ? ph + chunk * PARTB + 3 * PWB + i * 64 + g * 16 : OOB;
#pragma unroll
        for (int q = 0; q < 2; ++q) __builtin_amdgcn_raw_buffer_store_b128(pack2(acc[2 * q], acc[2 * q + 1]), rp, pv + q * 1024, 0, AUX_SC1);
        __builtin_amdgcn_raw_buffer_store_b128(__builtin_bit_cast(u32x4, accz), rp, pz, 0, AUX_SC1);
        unsigned fown = f0[0];
#pragma unroll
        for (int p = 1; p < 8; ++p) fown = (chunk == p) ? f0[p] : fown;
        const unsigned fl_wr = l == 0 ? hs + chunk * 4 : OOB;
        unsigned a0 = (lc < 2) ? (i >> 3) * 2048 + (16 * g + 8 * lc + (i & 7)) * 16 : 4 * 1024 + l * 16;
        asm volatile("" : "+v"(a0));
        u32x2 eye = {(i >> 1) == 2 * g ? ((i & 1) ? 0x3f800000u : 0x3f80u) : 0u, (i >> 1) == 2 * g + 1 ? ((i & 1) ? 0x3f800000u : 0x3f80u) : 0u};
        asm volatile("" : "+v"(eye));
        wait_vm<0>();
        __builtin_amdgcn_raw_buffer_store_b32(fown + 1u, rp, fl_wr, 0, AUX_SC1);
#pragma unroll 1
        for (unsigned spins = 0; spins < (1u << 18); ++spins) {
            const u32x8 f = flags_load(rp, hs);
            bool all = true;
#pragma unroll
            for (int p = 0; p < 8; ++p) all = all && (f[p] != f0[p]);
            if (all) break;
        }
        __builtin_amdgcn_raw_buffer_store_b32(fown + 2u, rp, fl_wr, 0, AUX_SC1);
        SB();
        u32x4 G[7][2];
        accz = __builtin_bit_cast(f32x4, __builtin_amdgcn_raw_buffer_load_b128(rp, lc == 2 ? (l & 7) * PARTB + g * 16 : OOB, ph + 3 * PWB, AUX_SC1));
        SB();
        if (lc == 2) pace(tk0, PV0 + 8 * PVD + 11 * QD - 40);
        if (lc < 2) lds_wait_ge(ldsb + L_TK + 8, 8u);
#pragma unroll
        for (int p = 0; p < 7; ++p) {
            const unsigned po = ph + ((chunk + 1 + p) & 7) * PARTB;
            SB();
#pragma unroll
            for (int e = 0; e < 2; ++e) G[p][e] = __builtin_amdgcn_raw_buffer_load_b128(rp, l * 16 + e * 1024, po + lc * PWB, AUX_SC1);
        }
        asm volatile("" ::: "memory");
        SB();
#pragma unroll
        for (int r = 0; r < 4; ++r) {
            accz[r] += dppf<0xB1>(accz[r]);
            accz[r] += dppf<0x4E>(accz[r]);
            accz[r] += dppf<0x141>(accz[r]);
        }
        if (lc == 2) *(u32x2*)(lds + L_KVF + 6144 + l * 8) = u32x2{pk_bf16(accz[0], accz[1]), pk_bf16(accz[2], accz[3])};
#pragma unroll
        for (int p = 0; p < 7; ++p)
#pragma unroll
            for (int e = 0; e < 4; ++e) {
                const unsigned u0 = G[p][e >> 1][2 * (e & 1)], u1 = G[p][e >> 1][2 * (e & 1) + 1];
                if (e < 3) acc[e] = mfma16(eye, u32x2{u0, u1}, acc[e]);
                else acc[e] += f32x4{bf_lo(u0), bf_hi(u0), bf_lo(u1), bf_hi(u1)};
            }
#pragma unroll
        for (int q = 0; q < 2; ++q) *(u32x4*)(lds + L_KVF + a0 + q * 1024) = pack2(acc[2 * q], acc[2 * q + 1]);
        if (lc == 2) {
            asm volatile("s_waitcnt lgkmcnt(0)\n\tds_write_b32 %0, %1" :: "v"(ldsb + L_TK + 4), "v"(1u) : "memory");
        } else {
            BAR();
        }
    }
}

extern "C" void kernel_launch(void* const* d_in, const int* in_sizes, int n_in,
                              void* d_out, int out_size, void* d_ws, size_t ws_size,
                              hipStream_t stream) {
    rfa_fused<<<H * NCH, NTHR, 0, stream>>>(d_in[0], d_in[1], d_in[2], d_in[3], d_out, d_ws);
}
```

```cpp
#include <hip/hip_runtime.h>
#include <stdint.h>
#include <type_traits>

typedef __attribute__((ext_vector_type(8))) __bf16 bf16x8;
typedef __attribute__((ext_vector_type(4))) short s16x4;
typedef __attribute__((ext_vector_type(2))) __bf16 bf16x2;
typedef __attribute__((ext_vector_type(4))) float f32x4;
typedef __attribute__((ext_vector_type(2))) float f32x2;
typedef __attribute__((ext_vector_type(2))) unsigned u32x2;
typedef __attribute__((ext_vector_type(4))) unsigned u32x4;
typedef __attribute__((ext_vector_type(8))) unsigned u32x8;
typedef __attribute__((ext_vector_type(16))) unsigned u32x16;
typedef __attribute__((address_space(4))) const unsigned long long* KARG;
typedef __attribute__((address_space(1))) unsigned gu32;
#define RLX_AGENT __ATOMIC_RELAXED, __HIP_MEMORY_SCOPE_AGENT
#define LAS __attribute__((address_space(3)))
#define SB() __builtin_amdgcn_sched_barrier(0)
#define BAR() asm volatile("s_waitcnt lgkmcnt(0)\n\ts_barrier" ::: "memory")

constexpr int H = 32, S = 2048, D = 192;
constexpr unsigned ROWB = D * 2;
constexpr unsigned NB = (unsigned)H * S * ROWB;
constexpr int NCH = 8;
constexpr int NTHR = 11 * 64;
constexpr float SCALE = (float)0.2686424829558855;
constexpr unsigned PV0 = 62;
constexpr unsigned PVD = 46;
constexpr unsigned QD = 32;
constexpr unsigned LIM = 40;
constexpr unsigned TILEB = 16 * ROWB;
constexpr unsigned L_OM = 16 * TILEB;
constexpr unsigned L_PHI = L_OM + TILEB;
constexpr unsigned L_KVF = L_PHI + 8192;
constexpr unsigned L_TK = L_KVF + 6 * 1024 + 512;
constexpr unsigned L_END = L_TK + 16;
constexpr unsigned PWB = 2048;
constexpr unsigned PARTB = 3 * PWB + 128;
constexpr unsigned W_HS = H * NCH * PARTB;
constexpr unsigned W_TK = W_HS + H * 3 * 128;
constexpr unsigned W_END = W_TK + 128;
constexpr unsigned OOB = 0x40000000u;
constexpr unsigned CODE_BYTES = 11648;
constexpr int AUX_NT = 2, AUX_SC1 = 16;

__device__ __forceinline__ unsigned pk_bf16(float lo, float hi) {
    f32x2 v = {lo, hi};
    return __builtin_bit_cast(unsigned, __builtin_convertvector(v, bf16x2));
}
__device__ __forceinline__ float bf_lo(unsigned u) { return __uint_as_float(u << 16); }
__device__ __forceinline__ float bf_hi(unsigned u) { return __uint_as_float(u & 0xffff0000u); }
__device__ __forceinline__ f32x4 rbf4(f32x4 x) {
    const unsigned a = pk_bf16(x[0], x[1]), b = pk_bf16(x[2], x[3]);
    return f32x4{bf_lo(a), bf_hi(a), bf_lo(b), bf_hi(b)};
}
__device__ __forceinline__ u32x4 scale4(u32x4 u, float sc) {
    f32x2 p[4];
#pragma unroll
    for (int c = 0; c < 4; ++c) p[c] = f32x2{bf_lo(u[c]), bf_hi(u[c])};
    SB();
#pragma unroll
    for (int c = 0; c < 4; ++c) p[c] *= f32x2{sc, sc};
    SB();
    return u32x4{pk_bf16(p[0][0], p[0][1]), pk_bf16(p[1][0], p[1][1]), pk_bf16(p[2][0], p[2][1]), pk_bf16(p[3][0], p[3][1])};
}
__device__ __forceinline__ u32x4 pack2(f32x4 a, f32x4 b) {
    return u32x4{pk_bf16(a[0], a[1]), pk_bf16(a[2], a[3]), pk_bf16(b[0], b[1]), pk_bf16(b[2], b[3])};
}
__device__ __forceinline__ f32x4 mfma32(u32x4 a, u32x4 b, f32x4 c) {
    return __builtin_amdgcn_mfma_f32_16x16x32_bf16(__builtin_bit_cast(bf16x8, a), __builtin_bit_cast(bf16x8, b), c, 0, 0, 0);
}
__device__ __forceinline__ f32x4 mfma16(u32x2 a, u32x2 b, f32x4 c) {
    return __builtin_amdgcn_mfma_f32_16x16x16bf16_1k(__builtin_bit_cast(s16x4, a), __builtin_bit_cast(s16x4, b), c, 0, 0, 0);
}
template <int MASK> __device__ __forceinline__ unsigned ror8(unsigned old, unsigned src) {
    return (unsigned)__builtin_amdgcn_update_dpp((int)old, (int)src, 0x128, 0xf, MASK, false);
}
template <int CTRL> __device__ __forceinline__ float dppf(float x) {
    return __builtin_bit_cast(float, __builtin_amdgcn_mov_dpp(__builtin_bit_cast(int, x), CTRL, 0xf, 0xf, true));
}
__device__ __forceinline__ unsigned lane_id() {
    unsigned l;
    asm volatile("v_mbcnt_lo_u32_b32 %0, -1, 0\n\tv_mbcnt_hi_u32_b32 %0, -1, %0" : "=v"(l));
    return l;
}
template <int N> __device__ __forceinline__ void dma_one(__amdgpu_buffer_rsrc_t r, unsigned ldsaddr, unsigned soff, unsigned vA, unsigned vB) {
    constexpr int T = N / 6, LC = (N % 6) / 2, RG = N & 1;
    asm volatile(
        "s_mov_b32 m0, %0\n\ts_nop 0\n\t"
        "buffer_load_dwordx4 %1, %2, %3 offen offset:%4 nt lds"
        :: "s"(ldsaddr + T * TILEB + LC * 896), "v"(RG ? vB : vA), "s"(r), "s"(soff + T * TILEB), "i"(RG * 3072 + LC * 128) : "m0", "memory");
}
__device__ __forceinline__ u32x8 flags_load(__amdgpu_buffer_rsrc_t r, unsigned off) {
    u32x8 x;
    asm volatile("s_buffer_load_dwordx8 %0, %1, %2 glc\n\ts_waitcnt lgkmcnt(0)" : "=&s"(x) : "s"(r), "s"(off) : "memory");
    return x;
}
__device__ __forceinline__ void pace(unsigned t0, unsigned dt) {
    unsigned d;
    asm volatile("s_branch 2f\n1:\n\ts_sleep 1\n2:\n\ts_memrealtime vcc\n\ts_waitcnt lgkmcnt(0)\n\ts_sub_u32 %0, vcc_lo, %1\n\ts_cmp_lt_u32 %0, %2\n\ts_cbranch_scc1 1b"
                 : "=&s"(d) : "s"(t0), "i"(dt) : "vcc", "scc", "memory");
}
__device__ __forceinline__ void pace_e(unsigned& t0, unsigned dt) {
    unsigned d;
    asm volatile("s_branch 2f\n1:\n\ts_sleep 1\n2:\n\ts_memrealtime vcc\n\ts_waitcnt lgkmcnt(0)\n\ts_sub_u32 %0, vcc_lo, %1\n\ts_cmp_lt_u32 %0, %2\n\ts_cbranch_scc1 1b\n\t"
                 "s_min_u32 %0, %0, %3\n\ts_sub_u32 %1, vcc_lo, %0"
                 : "=&s"(d), "+s"(t0) : "i"(dt), "i"(dt + LIM) : "vcc", "scc", "memory");
}
__device__ __forceinline__ void lds_wait_ge(unsigned addr, unsigned n) {
    unsigned c;
    asm volatile("s_branch 2f\n1:\n\ts_sleep 1\n2:\n\tds_read_b32 %0, %1\n\ts_waitcnt lgkmcnt(0)\n\tv_cmp_gt_u32 vcc, %2, %0\n\ts_cbranch_vccnz 1b"
                 : "=&v"(c) : "v"(addr), "v"(n) : "vcc", "memory");
}
template <int N> __device__ __forceinline__ void wait_vm() { SB(); asm volatile("s_waitcnt vmcnt(%0)" :: "i"(N) : "memory"); }
template <int B, int E, class F> __device__ __forceinline__ void static_for(F&& f) {
    if constexpr (B < E) { f(std::integral_constant<int, B>{}); static_for<B + 1, E>(f); }
}

__global__ void __launch_bounds__(NTHR, 3) rfa_fused(const void* Q_, const void* K_, const void* V_, const void* OM_, void* out_, void* ws_) {
    __shared__ __attribute__((aligned(16))) unsigned char lds[L_END];
    KARG ka = (KARG)__builtin_amdgcn_kernarg_segment_ptr();
    {
        unsigned vA, vB, s0_, s1_;
        asm volatile(
            "v_readfirstlane_b32 %[t0], %[tid]\n\t"
            "s_cmpk_lt_u32 %[t0], 192\n\t"
            "s_cselect_b32 %[t1], 24, 8\n\t"
            "s_load_dwordx2 s[40:41], %[ka], %[t1]\n\t"
            "v_bfe_u32 %[vA], %[tid], 3, 3\n\t"
            "v_lshlrev_b32 %[vB], 4, %[tid]\n\t"
            "v_mul_u32_u24 %[vA], 0x180, %[vA]\n\t"
            "v_bitop3_b32 %[vB], %[vB], %[tid], 48 bitop3:0x78\n\t"
            "v_and_b32 %[vB], 0x70, %[vB]\n\t"
            "v_or_b32 %[vA], %[vA], %[vB]\n\t"
            "v_xor_b32 %[vB], 64, %[vA]\n\t"
            "s_mov_b32 s42, -1\n\t"
            "s_bfm_b32 s43, 1, 17\n\t"
            "s_cbranch_scc1 2f\n\t"
            "s_lshr_b32 %[t0], %[t0], 6\n\t"
            "s_and_b32 %[t1], %[wg], 7\n\t"
            "s_addk_i32 %[t0], -3\n\t"
            "s_lshl_b32 %[t1], %[t1], 8\n\t"
            "s_add_u32 %[t1], %[t1], %[t0]\n\t"
            "s_mulk_i32 %[t0], 0x3000\n\t"
            "s_mov_b32 m0, %[t0]\n\t"
            "s_andn2_b32 %[t0], %[wg], 7\n\t"
            "s_add_u32 %[t1], %[t1], %[t0]\n\t"
            "s_mulk_i32 %[t1], 0x3000\n\t"
            "s_waitcnt lgkmcnt(0)\n\t"
            "buffer_load_dwordx4 %[vA], s[40:43], %[t1] offen nt lds\n\t"
            "buffer_load_dwordx4 %[vB], s[40:43], %[t1] offen offset:3072 nt lds\n\t"
            "s_addk_i32 m0, 896\n\ts_nop 0\n\t"
            "buffer_load_dwordx4 %[vA], s[40:43], %[t1] offen offset:128 nt lds\n\t"
            "buffer_load_dwordx4 %[vB], s[40:43], %[t1] offen offset:3200 nt lds\n\t"
            "s_addk_i32 m0, 896\n\ts_nop 0\n\t"
            "buffer_load_dwordx4 %[vA], s[40:43], %[t1] offen offset:256 nt lds\n\t"
            "buffer_load_dwordx4 %[vB], s[40:43], %[t1] offen offset:3328 nt lds\n\t"
            "s_addk_i32 m0, 4352\n\ts_addk_i32 %[t1], 6144\n\t"
            "buffer_load_dwordx4 %[vA], s[40:43], %[t1] offen nt lds\n\t"
            "buffer_load_dwordx4 %[vB], s[40:43], %[t1] offen offset:3072 nt lds\n\t"
            "s_addk_i32 m0, 896\n\ts_nop 0\n\t"
            "buffer_load_dwordx4 %[vA], s[40:43], %[t1] offen offset:128 nt lds\n\t"
            "buffer_load_dwordx4 %[vB], s[40:43], %[t1] offen offset:3200 nt lds\n\t"
            "s_addk_i32 m0, 896\n\ts_nop 0\n\t"
            "buffer_load_dwordx4 %[vA], s[40:43], %[t1] offen offset:256 nt lds\n\t"
            "buffer_load_dwordx4 %[vB], s[40:43], %[t1] offen offset:3328 nt lds\n\t"
            "s_branch 3f\n"
            "2:\n\t"
            "s_lshl_b32 %[t1], %[t0], 4\n\t"
            "s_add_u32 m0, %[t1], %[lom]\n\t"
            "s_lshl_b32 %[t1], %[t0], 1\n\t"
            "s_waitcnt lgkmcnt(0)\n\t"
            "buffer_load_dwordx4 %[vA], s[40:43], %[t1] offen lds\n\t"
            "buffer_load_dwordx4 %[vB], s[40:43], %[t1] offen offset:3072 lds\n"
            "3:"
            : [vA] "=&v"(vA), [vB] "=&v"(vB), [t0] "=&s"(s0_), [t1] "=&s"(s1_), [ka] "+s"(ka)
            : [tid] "v"(threadIdx.x), [wg] "s"(blockIdx.x), [lom] "i"(L_OM)
            : "s40", "s41", "s42", "s43", "m0", "scc", "memory");
    }
    const void* const Q = (const void*)ka[0];
    const void* const V = (const void*)ka[2];
    void* const out = (void*)ka[4];
    void* const ws = (void*)ka[5];
    unsigned long long pc0;
    asm volatile("s_getpc_b64 %0" : "=s"(pc0));
    const int b = blockIdx.x;
    const int head = (b & 7) * 4 + (b >> 6);
    const int chunk = (b >> 3) & 7;
    const int w = __builtin_amdgcn_readfirstlane(threadIdx.x >> 6);
    const unsigned ldsb = (unsigned)(size_t)(LAS unsigned char*)lds;
    const unsigned wgoff = ((unsigned)head * S + chunk * (S / NCH)) * ROWB;
    const f32x4 z4 = {0.f, 0.f, 0.f, 0.f};

    const unsigned hs = W_HS + (unsigned)(head * 3 + w) * 128;
    if (w < 3) {
        {
            const unsigned long long pc = (pc0 & ~255ull) - 256u + min((unsigned)((b >> 3) * 3 + w) * 128u, CODE_BYTES - 128u);
            unsigned c0, c1, c2;
            asm volatile("s_load_dword %0, %3, 0x0\n\ts_load_dword %1, %3, 0x40\n\ts_load_dword %2, %4, 0x0\n\ts_waitcnt lgkmcnt(0)"
                         : "=&s"(c0), "=&s"(c1), "=&s"(c2) : "s"(pc), "s"((const char*)ws + W_END) : "memory");
        }
        wait_vm<0>();
        if (w == 0) *(u32x4*)(lds + L_TK) = u32x4{0u, 0u, 0u, 0u};
    }
    BAR();
    int w2 = w;
    asm volatile("" : "+s"(w2));

    if (w2 >= 3) {
        const int kw = w - 3;
        const unsigned l = lane_id();
        const unsigned i = l & 15, g = l >> 4;
        const unsigned vA = (l >> 3) * ROWB + (((l & 7) ^ (l >> 4)) << 4), vB = vA ^ 64u;
        const unsigned woff = wgoff + kw * 2 * TILEB, lws = kw * 2 * TILEB;
        const unsigned fe = (i >> 3) * 3072 + (i & 7) * 128 + ((g ^ (i >> 1)) << 4), fo = fe ^ 64u;
        const unsigned phw = L_PHI + kw * 1024 + i * 32 + g * 8;
        if (kw & 1) __builtin_amdgcn_s_sleep(6);
        if (kw & 2) __builtin_amdgcn_s_sleep(12);
        if (kw & 4) __builtin_amdgcn_s_sleep(24);
        u32x4 wf[6];
#pragma unroll
        for (int t = 0; t < 6; ++t) wf[t] = *(const u32x4*)(lds + L_OM + ((t & 1) ? fo : fe) + (t >> 1) * 1024);
        const __amdgpu_buffer_rsrc_t rq = __builtin_amdgcn_make_buffer_rsrc((void*)Q, 0, NB, 0x00020000);
        const __amdgpu_buffer_rsrc_t ro = __builtin_amdgcn_make_buffer_rsrc(out, 0, NB, 0x00020000);
        float scale = SCALE;
        asm volatile("" : "+s"(scale));
        u32x2 zf, pq;
        u32x4 kf[6];
        auto out_tile = [&](const int T) __attribute__((always_inline)) {
            const unsigned vo1 = woff + T * TILEB + g * 4 * ROWB + i * 16;
            unsigned vo2 = woff + T * TILEB + (g * 4 + (i >> 3)) * ROWB + 256 + (i & 7) * 16;
            f32x4 dn = mfma16(pq, zf, z4);
            SB();
            f32x4 n[12];
#pragma unroll
            for (int t = 4; t < 6; ++t) {
                n[2 * t] = mfma16(pq, u32x2{kf[t][0], kf[t][1]}, z4);
                n[2 * t + 1] = mfma16(pq, u32x2{kf[t][2], kf[t][3]}, z4);
            }
            SB();
            dn = rbf4(dn);
            dn = rbf4(dn + 1e-6f);
            float rd[4];
#pragma unroll
            for (int r = 0; r < 4; ++r) rd[r] = __builtin_amdgcn_rcpf(dn[r] * 0.25f) * 0.25f;
            SB();
            u32x4 xt[2];
#pragma unroll
            for (int r = 0; r < 4; r += 2) {
                unsigned o[2][2];
#pragma unroll
                for (int u = 0; u < 2; ++u)
#pragma unroll
                    for (int c = 0; c < 2; ++c) {
                        const unsigned y = pk_bf16(n[8 + 2 * c][r + u], n[9 + 2 * c][r + u]);
                        o[u][c] = pk_bf16(bf_lo(y) * rd[r + u], bf_hi(y) * rd[r + u]);
                    }
                xt[r / 2] = u32x4{ror8<0xC>(o[0][0], o[1][0]), ror8<0xC>(o[0][1], o[1][1]), ror8<0x3>(o[1][0], o[0][0]), ror8<0x3>(o[1][1], o[0][1])};
            }
            asm volatile("" : "+v"(xt[0]), "+v"(xt[1]), "+v"(vo2));
            if (T == 0) {
                wait_vm<0>();
                BAR();
#pragma unroll
                for (int t = 0; t < 4; ++t) kf[t] = *(const u32x4*)(lds + L_KVF + t * 1024 + l * 16);
            }
            SB();
            __builtin_amdgcn_raw_buffer_store_b128(xt[0], ro, vo2, 0, AUX_SC1);
            __builtin_amdgcn_raw_buffer_store_b128(xt[1], ro, vo2 + 2 * ROWB, 0, AUX_SC1);
            SB();
#pragma unroll
            for (int t = 0; t < 4; ++t) {
                n[2 * t] = mfma16(pq, u32x2{kf[t][0], kf[t][1]}, z4);
                n[2 * t + 1] = mfma16(pq, u32x2{kf[t][2], kf[t][3]}, z4);
            }
            SB();
#pragma unroll
            for (int r = 0; r < 4; ++r) {
                u32x4 o = scale4(u32x4{pk_bf16(n[0][r], n[1][r]), pk_bf16(n[2][r], n[3][r]), pk_bf16(n[4][r], n[5][r]), pk_bf16(n[6][r], n[7][r])}, rd[r]);
                asm volatile("" : "+v"(o));
                __builtin_amdgcn_raw_buffer_store_b128(o, ro, vo1 + r * ROWB, 0, AUX_SC1);
                SB();
            }
        };
        unsigned tk = 0;
        auto q_issue = [&](auto nc) __attribute__((always_inline)) {
            constexpr int N = decltype(nc)::value;
            pace_e(tk, PV0 + 8 * PVD + N * QD);
            dma_one<N>(rq, ldsb + lws, woff, vA, vB);
            if (N == 11) asm volatile("s_mov_b64 exec, 1\n\tds_add_u32 %0, %1\n\ts_mov_b64 exec, -1" :: "v"(ldsb + L_TK + 8), "v"(1u) : "memory");
        };
#pragma unroll 1
        for (int it = 0;; ++it) {
            if (it == 2) {
                if ((kw == 0 || kw == 2) && (chunk == 0 || chunk == 7)) {
                    const char* tp = (const char*)(kw == 0 ? Q : (const void*)out) + wgoff + (chunk & 1) * (16 * TILEB - 128);
                    unsigned x;
                    asm volatile("s_load_dword %0, %1, 0x0\n\ts_waitcnt lgkmcnt(0)" : "=&s"(x) : "s"(tp) : "memory");
                }
                BAR();
                tk = __builtin_amdgcn_readfirstlane(*(const unsigned*)(lds + L_TK)) + kw * 4 - 14;
                static_for<0, 4>(q_issue);
            }
            if (it >= 3) {
                if (it == 3) {
                    static_for<10, 12>(q_issue);
                    lds_wait_ge(ldsb + L_TK + 4, 1u);
                    zf = *(const u32x2*)(lds + L_KVF + 6144 + l * 8);
                    kf[4] = *(const u32x4*)(lds + L_KVF + 4 * 1024 + l * 16);
                    kf[5] = *(const u32x4*)(lds + L_KVF + 5 * 1024 + l * 16);
                    __builtin_amdgcn_s_setprio(2);
                } else {
                    __builtin_amdgcn_s_setprio(0);
                }
                out_tile(it - 3);
                if (it == 4) break;
            }
            f32x4 pr = z4, gr = z4;
            const unsigned tb = lws + (it & 1) * TILEB;
            static_for<0, 3>([&](auto jc) __attribute__((always_inline)) {
                constexpr int J = decltype(jc)::value;
                if (it == 2) { static_for<4 + 2 * J, 6 + 2 * J>(q_issue); wait_vm<4>(); }
                wait_vm<10 - 2 * J>();
                if (it == 1) wait_vm<4 - 2 * J>();
                const u32x4 r0 = *(const u32x4*)(lds + tb + fe + J * 1024);
                const u32x4 r1 = *(const u32x4*)(lds + tb + fo + J * 1024);
                const u32x4 x0 = scale4(r0, scale);
                pr = mfma32(wf[2 * J], x0, pr);
                gr = mfma32(x0, x0, gr);
                const u32x4 x1 = scale4(r1, scale);
                gr = mfma32(x1, x1, gr);
                pr = mfma32(wf[2 * J + 1], x1, pr);
            });
            float dg = (i & 2) ? ((i & 1) ? gr[3] : gr[2]) : ((i & 1) ? gr[1] : gr[0]);
            dg = ((i >> 2) == g) ? dg : 0.f;
            const f32x4 sqb = __builtin_amdgcn_mfma_f32_16x16x4f32(1.0f, dg, z4, 0, 0, 0);
            const f32x4 pj = rbf4(pr);
            const float sq = bf_lo(pk_bf16(sqb[0], sqb[0]));
            f32x4 x;
#pragma unroll
            for (int r = 0; r < 4; ++r) x[r] = __builtin_fmaf(sq, -0.5f, pj[r]);
            x = rbf4(x);
#pragma unroll
            for (int r = 0; r < 4; ++r) x[r] = __builtin_amdgcn_exp2f(x[r] * 1.4426950408889634f);
            x = rbf4(x);
#pragma unroll
            for (int r = 0; r < 4; ++r) x[r] = __builtin_fmaf(x[r], 0.25f, 1e-6f);
            pq = u32x2{pk_bf16(x[0], x[1]), pk_bf16(x[2], x[3])};
            if (it < 2) *(u32x2*)(lds + phw + it * 512) = pq;
        }
        __builtin_amdgcn_endpgm();
    } else {
        const unsigned l = lane_id();
        const unsigned i = l & 15, g = l >> 4;
        const int lc = w;
        const __amdgpu_buffer_rsrc_t rp = __builtin_amdgcn_make_buffer_rsrc(ws, 0, W_END, 0x00020000);
        const __amdgpu_buffer_rsrc_t rv = __builtin_amdgcn_make_buffer_rsrc((void*)V, 0, NB, 0x00020000);
        const unsigned vv = (4 * g + (i >> 3)) * ROWB + (i & 7) * 16;
        const unsigned sv = wgoff + lc * 128;
        unsigned tk0 = (unsigned)__builtin_amdgcn_s_memrealtime();
        asm volatile("buffer_store_dword %0, %1, %2, 0 offen sc1"
                     :: "v"(tk0), "v"((l == 0 && lc == 0 && (b & 0x78) == 0) ? W_TK + ((b & 7) | ((b >> 7) << 3)) * 4 : OOB), "s"(rp) : "memory");
        const unsigned f0 = __hip_atomic_load((gu32*)((unsigned char*)ws + hs) + (l & 7), RLX_AGENT);
        u32x4 L[8][4];
        auto prep = [&](const int ks) __attribute__((always_inline)) {
            u32x4 bf[4];
#pragma unroll
            for (int p = 0; p < 4; ++p)
#pragma unroll
                for (int h = 0; h < 2; ++h) {
                    const unsigned X = ror8<0xC>(L[ks][p][h], L[ks][p][2 + h]);
                    const unsigned Y = ror8<0x3>(L[ks][p][2 + h], L[ks][p][h]);
                    bf[2 * h][p] = __builtin_amdgcn_perm(Y, X, 0x05040100u);
                    bf[2 * h + 1][p] = __builtin_amdgcn_perm(Y, X, 0x07060302u);
                }
#pragma unroll
            for (int e = 0; e < 4; ++e) L[ks][e] = bf[e];
            asm volatile("" : "+v"(L[ks][0]), "+v"(L[ks][1]), "+v"(L[ks][2]), "+v"(L[ks][3]));
        };
        SB();
#pragma unroll
        for (int ks = 0; ks < 8; ++ks) {
            if (ks > 2) pace_e(tk0, PV0 + ks * PVD); else pace(tk0, PV0 + ks * PVD);
#pragma unroll
            for (int a = 0; a < 4; ++a)
                L[ks][a] = __builtin_amdgcn_raw_buffer_load_b128(rv, vv + (a & 1) * 2 * ROWB, sv + ((ks + chunk) & 7) * 2 * TILEB + (a >> 1) * TILEB, AUX_NT);
            SB();
            if (ks == 2) {
                u32x16 ts;
                asm volatile("s_buffer_load_dwordx16 %0, %1, %2\n\ts_waitcnt lgkmcnt(0)" : "=&s"(ts) : "s"(rp), "s"(W_TK) : "memory");
                unsigned sum = 0;
#pragma unroll
                for (int p = 0; p < 16; ++p) { const unsigned d = ts[p] - tk0 + 256u; sum += d < 512u ? d : 256u; }
                tk0 += (sum >> 4) - 256u;
                if (lc == 0) *(unsigned*)(lds + L_TK) = tk0;
                SB();
            }
            if (ks >= 2 && ks < 7) { prep(ks - 2); SB(); }
        }
        BAR();
        __builtin_amdgcn_s_setprio(3);
        f32x4 acc[4] = {z4, z4, z4, z4}, accz = z4;
        const u32x4 ones = {0x3f803f80u, 0x3f803f80u, 0x3f803f80u, 0x3f803f80u};
#pragma unroll
        for (int ks = 0; ks < 8; ++ks) {
            LAS unsigned char* pp = (LAS unsigned char*)lds + L_PHI + ((ks + chunk) & 7) * 1024 + l * 8;
            const u32x2 p0 = __builtin_bit_cast(u32x2, __builtin_amdgcn_ds_read_tr16_b64_v4i16((LAS s16x4*)pp));
            const u32x2 p1 = __builtin_bit_cast(u32x2, __builtin_amdgcn_ds_read_tr16_b64_v4i16((LAS s16x4*)(pp + 512)));
            const u32x4 pa = {p0[0], p0[1], p1[0], p1[1]};
            if (ks >= 5) { SB(); prep(ks); }
            accz = mfma32(pa, ones, accz);
#pragma unroll
            for (int e = 0; e < 4; ++e) acc[e] = mfma32(pa, L[ks][e], acc[e]);
        }
        const unsigned ph = (unsigned)head * NCH * PARTB;
        const unsigned pv = ph + chunk * PARTB + lc * PWB + l * 16;
        const unsigned pz = (i <= 1 && lc == 2) ? ph + chunk * PARTB + 3 * PWB + i * 64 + g * 16 : OOB;
#pragma unroll
        for (int q = 0; q < 2; ++q) __builtin_amdgcn_raw_buffer_store_b128(pack2(acc[2 * q], acc[2 * q + 1]), rp, pv + q * 1024, 0, AUX_SC1);
        __builtin_amdgcn_raw_buffer_store_b128(__builtin_bit_cast(u32x4, accz), rp, pz, 0, AUX_SC1);
        const unsigned fown = __builtin_amdgcn_readlane(f0, chunk);
        u32x8 f1;
#pragma unroll
        for (int p = 0; p < 8; ++p) f1[p] = __builtin_amdgcn_readlane(f0, p) + 1u;
        asm volatile("" : "+s"(f1));
        const unsigned fl_wr = l == 0 ? hs + chunk * 4 : OOB;
        unsigned a0 = (lc < 2) ? (i >> 3) * 2048 + (16 * g + 8 * lc + (i & 7)) * 16 : 4 * 1024 + l * 16;
        asm volatile("" : "+v"(a0));
        u32x2 eye = {(i >> 1) == 2 * g ? ((i & 1) ? 0x3f800000u : 0x3f80u) : 0u, (i >> 1) == 2 * g + 1 ? ((i & 1) ? 0x3f800000u : 0x3f80u) : 0u};
        asm volatile("" : "+v"(eye));
        wait_vm<0>();
        __builtin_amdgcn_raw_buffer_store_b32(fown + 1u, rp, fl_wr, 0, AUX_SC1);
        bool all = false;
#pragma unroll 1
        for (unsigned spins = 0; spins < 32u && !all; ++spins) {
            const u32x8 f = flags_load(rp, hs);
            unsigned far = 0;
#pragma unroll
            for (int p = 0; p < 8; ++p) far |= f[p] - f1[p];
            all = far < 2u;
        }
#pragma unroll 1
        for (unsigned spins = 0; spins < (1u << 18) && !all; ++spins)
            all = __all(__hip_atomic_load((gu32*)((unsigned char*)ws + hs) + (l & 7), RLX_AGENT) - f0 - 1u < 2u);
        asm volatile("" ::: "memory");
        __builtin_amdgcn_raw_buffer_store_b32(fown + 2u, rp, fl_wr, 0, AUX_SC1);
        SB();
        u32x4 G[7][2];
        accz = __builtin_bit_cast(f32x4, __builtin_amdgcn_raw_buffer_load_b128(rp, lc == 2 ? (l & 7) * PARTB + g * 16 : OOB, ph + 3 * PWB, AUX_SC1));
        SB();
        if (lc == 2) pace(tk0, PV0 + 8 * PVD + 11 * QD - 40);
        if (lc < 2) lds_wait_ge(ldsb + L_TK + 8, 8u);
#pragma unroll
        for (int p = 0; p < 7; ++p) {
            const unsigned po = ph + ((chunk + 1 + p) & 7) * PARTB;
            SB();
#pragma unroll
            for (int e = 0; e < 2; ++e) G[p][e] = __builtin_amdgcn_raw_buffer_load_b128(rp, l * 16 + e * 1024, po + lc * PWB, AUX_SC1);
        }
        asm volatile("" ::: "memory");
        SB();
#pragma unroll
        for (int r = 0; r < 4; ++r) {
            accz[r] += dppf<0xB1>(accz[r]);
            accz[r] += dppf<0x4E>(accz[r]);
            accz[r] += dppf<0x141>(accz[r]);
        }
        if (lc == 2) *(u32x2*)(lds + L_KVF + 6144 + l * 8) = u32x2{pk_bf16(accz[0], accz[1]), pk_bf16(accz[2], accz[3])};
#pragma unroll
        for (int p = 0; p < 7; ++p)
#pragma unroll
            for (int e = 0; e < 4; ++e) {
                const unsigned u0 = G[p][e >> 1][2 * (e & 1)], u1 = G[p][e >> 1][2 * (e & 1) + 1];
                if (e < 3) acc[e] = mfma16(eye, u32x2{u0, u1}, acc[e]);
                else acc[e] += f32x4{bf_lo(u0), bf_hi(u0), bf_lo(u1), bf_hi(u1)};
            }
#pragma unroll
        for (int q = 0; q < 2; ++q) *(u32x4*)(lds + L_KVF + a0 + q * 1024) = pack2(acc[2 * q], acc[2 * q + 1]);
        if (lc == 2) {
            asm volatile("s_waitcnt lgkmcnt(0)\n\tds_write_b32 %0, %1" :: "v"(ldsb + L_TK + 4), "v"(1u) : "memory");
        } else {
            BAR();
        }
    }
}

extern "C" void kernel_launch(void* const* d_in, const int* in_sizes, int n_in,
                              void* d_out, int out_size, void* d_ws, size_t ws_size,
                              hipStream_t stream) {
    rfa_fused<<<H * NCH, NTHR, 0, stream>>>(d_in[0], d_in[1], d_in[2], d_in[3], d_out, d_ws);
}
```
